```python
import torch

def _softmax(x, axis=-1):
    return torch.softmax(x, dim=axis)
def _gelu(x):
    return torch.nn.functional.gelu(x, approximate="tanh")
def _silu(x):
    return torch.nn.functional.silu(x)
def _sigmoid(x):
    return torch.sigmoid(x)
def _layernorm(x, axis=-1, eps=1e-5):
    m = x.mean(dim=axis, keepdim=True)
    v = x.var(dim=axis, keepdim=True, unbiased=False)
    return (x - m) / torch.sqrt(v + eps)
def _scatter_add(tgt, idx, src, axis=0):
    return tgt.clone().index_add_(axis, idx, src)
def _segment_sum(src, seg, n):
    out = torch.zeros((n,) + src.shape[1:], dtype=src.dtype)
    return out.index_add_(0, seg, src)
def _pad(x, pw):
    flat = tuple(v for pair in reversed(pw) for v in pair)
    return torch.nn.functional.pad(x, flat)

kernel_name = 'recurrent_scan__ssm_conv_s4d__dense_activation_silu__prompt'

def setup_inputs():

    _w = (lambda a: (lambda t: t.to(torch.bfloat16) if t.is_floating_point() else t)(torch.as_tensor(a)))
    def _A_setup():
        Bsz, L, D, N = 1, 2048, 512, 128
        return {
            "u": _w((lambda *s: torch.randn(*s, dtype=torch.bfloat16) * 0.05)(Bsz, L, D)),
            "log_A_real": _w((lambda *s: torch.randn(*s, dtype=torch.bfloat16) * 6.4)(D, N) * 0.5),
            "A_imag": _w((lambda *s: torch.randn(*s, dtype=torch.bfloat16) * 0.2)(D, N) * 3.14),
            "C_real": _w((lambda *s: torch.randn(*s, dtype=torch.bfloat16) * 0.4)(D, N) / N ** 0.5),
            "C_imag": _w((lambda *s: torch.randn(*s, dtype=torch.bfloat16) * 0.4)(D, N) / N ** 0.5),
            "log_dt": _w((lambda *s: torch.randn(*s, dtype=torch.bfloat16) * 0.2)(D) * 0.5 - 4.0),
            "D_skip": _w((lambda *s: torch.randn(*s, dtype=torch.bfloat16) * 0.2)(D)),
        }
    _A_in = _A_setup()

    def _B_setup():
        B, S, D, F = 1, 2048, 512, 1792
        return {
            "x": _w((lambda *s: torch.randn(*s, dtype=torch.bfloat16) * 0.05)(B, S, D)),
            "w1": _w((lambda *s: torch.randn(*s, dtype=torch.bfloat16) * 0.8)(D, F) * (D ** -0.5)),
            "w2": _w((lambda *s: torch.randn(*s, dtype=torch.bfloat16) * 0.1)(F, D) * (F ** -0.5)),
        }
    _B_in = _B_setup()

    return {
        "A_u": _A_in["u"],
        "A_log_A_real": _A_in["log_A_real"],
        "A_A_imag": _A_in["A_imag"],
        "A_C_real": _A_in["C_real"],
        "A_C_imag": _A_in["C_imag"],
        "A_log_dt": _A_in["log_dt"],
        "A_D_skip": _A_in["D_skip"],
        "B_x": _B_in["x"],
        "B_w1": _B_in["w1"],
        "B_w2": _B_in["w2"],
    }

def reference(A_u, A_log_A_real, A_A_imag, A_C_real, A_C_imag, A_log_dt, A_D_skip, B_x, B_w1, B_w2):
    def _A_ref(u, log_A_real, A_imag, C_real, C_imag, log_dt, D_skip):
        L = u.shape[1]
        dt = torch.exp(log_dt)
        A = -torch.exp(log_A_real) + 1j * A_imag
        C = (C_real + 1j * C_imag) * dt[:, None]
        pos = (lambda x, dt: x.to(dt))(torch.arange(L), log_dt.dtype)
        P = torch.exp((A * dt[:, None])[:, :, None] * pos[None, None, :])
        K = 2.0 * torch.einsum("dn,dnl->dl", C, P).real
        L2 = 2 * L
        u_t = (lambda x, *d: x.permute(*d))(u, 0, 2, 1)
        Kf = (lambda x, *a, **k: torch.fft.rfft(x.float(), *a, **k))(K, L2)
        Uf = (lambda x, *a, **k: torch.fft.rfft(x.float(), *a, **k))(u_t, L2)
        y = (lambda x, *a, **k: torch.fft.irfft(x, *a, **k).to(torch.bfloat16))(Uf * Kf[None, :, :], L2)[:, :, :L]
        y = (lambda x, *d: x.permute(*d))(y, 0, 2, 1)
        return y + u * D_skip[None, None, :]

    def _B_ref(x, w1, w2):
        h = torch.einsum("bsd,df->bsf", x, w1)
        h = _silu(h)
        out = torch.einsum("bsf,fd->bsd", h, w2)
        return out

    x = A_u
    xn = _layernorm(x, axis=-1)
    s = _A_ref(xn, A_log_A_real, A_A_imag, A_C_real, A_C_imag, A_log_dt, A_D_skip)
    g = _silu(_B_ref(xn, B_w1, B_w2))
    return x + s * g
```

```cpp
#include <hip/hip_runtime.h>
#include <hip/hip_bf16.h>
#include <stdint.h>

typedef unsigned short u16;
constexpr int L = 2048, D = 512, N = 128, F = 1792;

__device__ __forceinline__ float bf2f(u16 v) { return __uint_as_float(((unsigned)v) << 16); }
__device__ __forceinline__ u16 f2bf(float f) {
    unsigned u = __float_as_uint(f);
    u += 0x7FFF + ((u >> 16) & 1);
    return (u16)(u >> 16);
}
__device__ __forceinline__ float rbf(float f) { return bf2f(f2bf(f)); }

__global__ void ln_kernel(const u16* x, u16* xn) {
    int row = blockIdx.x;
    int t = threadIdx.x;
    float v[8];
    float s = 0;
    for (int i = 0; i < 8; ++i) { v[i] = bf2f(x[row * D + t * 8 + i]); s += v[i]; }
    for (int o = 32; o; o >>= 1) s += __shfl_xor(s, o);
    float mean = s / D;
    float q = 0;
    for (int i = 0; i < 8; ++i) { float d = v[i] - mean; q += d * d; }
    for (int o = 32; o; o >>= 1) q += __shfl_xor(q, o);
    float var = rbf(q / D);
    float m = rbf(mean);
    float sd = rbf(sqrtf(rbf(var + 1e-5f)));
    for (int i = 0; i < 8; ++i) xn[row * D + t * 8 + i] = f2bf(rbf(v[i] - m) / sd);
}

__global__ void kgen_kernel(const u16* lar, const u16* aim, const u16* cr, const u16* ci, const u16* ldt, float* K) {
    int d = blockIdx.x;
    float dt = rbf(expf(bf2f(ldt[d])));
    for (int l = threadIdx.x; l < L; l += blockDim.x) {
        float pos = rbf((float)l);
        float acc = 0;
        for (int n = 0; n < N; ++n) {
            float ar = -rbf(expf(bf2f(lar[d * N + n]))) * dt;
            float ai = bf2f(aim[d * N + n]) * dt;
            float c_r = bf2f(cr[d * N + n]) * dt, c_i = bf2f(ci[d * N + n]) * dt;
            float e = expf(ar * pos);
            float sn, cs;
            sincosf(ai * pos, &sn, &cs);
            acc += c_r * (e * cs) - c_i * (e * sn);
        }
        K[d * L + l] = 2.f * acc;
    }
}

__global__ void conv_kernel(const u16* xn, const float* K, const u16* dskip, u16* s) {
    int d = blockIdx.x;
    __shared__ float xs[L], ks[L];
    for (int l = threadIdx.x; l < L; l += blockDim.x) { xs[l] = bf2f(xn[l * D + d]); ks[l] = K[d * L + l]; }
    __syncthreads();
    float ds = bf2f(dskip[d]);
    for (int l = threadIdx.x; l < L; l += blockDim.x) {
        float acc = 0;
        for (int j = 0; j <= l; ++j) acc += ks[j] * xs[l - j];
        s[l * D + d] = f2bf(rbf(acc) + rbf(xs[l] * ds));
    }
}

__global__ void gemm1_kernel(const u16* xn, const u16* w1, u16* h) {
    int row = blockIdx.x;
    __shared__ float xs[D];
    for (int i = threadIdx.x; i < D; i += blockDim.x) xs[i] = bf2f(xn[row * D + i]);
    __syncthreads();
    for (int f = threadIdx.x; f < F; f += blockDim.x) {
        float acc = 0;
        for (int k = 0; k < D; ++k) acc += xs[k] * bf2f(w1[k * F + f]);
        float v = rbf(acc);
        h[row * F + f] = f2bf(v / (1.f + expf(-v)));
    }
}

__global__ void gemm2_kernel(const u16* h, const u16* w2, const u16* x, const u16* s, u16* out) {
    int row = blockIdx.x;
    __shared__ float hs[F];
    for (int i = threadIdx.x; i < F; i += blockDim.x) hs[i] = bf2f(h[row * F + i]);
    __syncthreads();
    for (int c = threadIdx.x; c < D; c += blockDim.x) {
        float acc = 0;
        for (int k = 0; k < F; ++k) acc += hs[k] * bf2f(w2[k * D + c]);
        float v = rbf(acc);
        float g = rbf(v / (1.f + expf(-v)));
        out[row * D + c] = f2bf(bf2f(x[row * D + c]) + rbf(bf2f(s[row * D + c]) * g));
    }
}

extern "C" void kernel_launch(void* const* d_in, const int* in_sizes, int n_in,
                              void* d_out, int out_size, void* d_ws, size_t ws_size,
                              hipStream_t stream) {
    const u16* x = (const u16*)d_in[0];
    const u16* lar = (const u16*)d_in[1];
    const u16* aim = (const u16*)d_in[2];
    const u16* cr = (const u16*)d_in[3];
    const u16* ci = (const u16*)d_in[4];
    const u16* ldt = (const u16*)d_in[5];
    const u16* dsk = (const u16*)d_in[6];
    const u16* w1 = (const u16*)d_in[8];
    const u16* w2 = (const u16*)d_in[9];
    char* ws = (char*)d_ws;
    u16* xn = (u16*)ws;
    float* K = (float*)(ws + (4 << 20));
    u16* s = (u16*)(ws + (8 << 20));
    u16* h = (u16*)(ws + (12 << 20));
    ln_kernel<<<L, 64, 0, stream>>>(x, xn);
    kgen_kernel<<<D, 256, 0, stream>>>(lar, aim, cr, ci, ldt, K);
    conv_kernel<<<D, 256, 0, stream>>>(xn, K, dsk, s);
    gemm1_kernel<<<L, 256, 0, stream>>>(xn, w1, h);
    gemm2_kernel<<<L, 256, 0, stream>>>(h, w2, x, s, (u16*)d_out);
}
```

```cpp
#include <hip/hip_runtime.h>
#include <hip/hip_bf16.h>
#include <stdint.h>

typedef unsigned short u16;
typedef unsigned v4u __attribute__((ext_vector_type(4)));
typedef unsigned v2u __attribute__((ext_vector_type(2)));
typedef float f32x2 __attribute__((ext_vector_type(2)));
typedef float f32x4 __attribute__((ext_vector_type(4)));
typedef float f32x16 __attribute__((ext_vector_type(16)));
typedef short s16x4 __attribute__((ext_vector_type(4)));
typedef __bf16 bf16x2 __attribute__((ext_vector_type(2)));
typedef __bf16 bf16x8 __attribute__((ext_vector_type(8)));
typedef _Float16 f16x2 __attribute__((ext_vector_type(2)));
typedef _Float16 f16x8 __attribute__((ext_vector_type(8)));
#define GAS __attribute__((address_space(1)))
#define LAS __attribute__((address_space(3)))
#define DEV __device__ __forceinline__

constexpr int L = 2048, D = 512, NM = 128, F = 1792;
constexpr int WS_XT = 1 << 20;
constexpr int WS_ST = WS_XT + (2 << 20);
constexpr int WS_HF = WS_ST + (2 << 20);
constexpr int WS_BYTES = WS_HF + L * F * 2;
constexpr int AUX_SC1 = 16;
constexpr int LDS_BYTES = 160 * 1024;

DEV float bf2f(unsigned v) { return __uint_as_float(v << 16); }
DEV float bflo(unsigned v) { return __uint_as_float(v << 16); }
DEV float bfhi(unsigned v) { return __uint_as_float(v & 0xffff0000u); }
DEV unsigned pk_bf16(float a, float b) { f32x2 f = {a, b}; return __builtin_bit_cast(unsigned, __builtin_convertvector(f, bf16x2)); }
DEV unsigned pk_f16(float a, float b) { f32x2 f = {a, b}; return __builtin_bit_cast(unsigned, __builtin_convertvector(f, f16x2)); }
DEV float rbf(float a) { return bflo(pk_bf16(a, a) & 0xffffu); }
DEV float dot2bf(unsigned a, unsigned b, float c) {
  return __builtin_amdgcn_fdot2_f32_bf16(__builtin_bit_cast(bf16x2, a), __builtin_bit_cast(bf16x2, b), c, false);
}
DEV float silu_f(float v) { return v * __builtin_amdgcn_rcpf(1.f + __builtin_amdgcn_exp2f(v * -1.44269504088896f)); }
DEV void raw_barrier() { asm volatile("s_waitcnt lgkmcnt(0)" ::: "memory"); __builtin_amdgcn_s_barrier(); asm volatile("" ::: "memory"); }
template <int CTRL> DEV float dpp_add(float v) {
  return v + __builtin_bit_cast(float, __builtin_amdgcn_update_dpp(0, __builtin_bit_cast(int, v), CTRL, 0xf, 0xf, true));
}
DEV float wave_sum(float v) {
  v = dpp_add<0xb1>(v);
  v = dpp_add<0x4e>(v);
  v = dpp_add<0x124>(v);
  v = dpp_add<0x128>(v);
  v += __shfl_xor(v, 16);
  v += __shfl_xor(v, 32);
  return v;
}

#define STAMP(p, i)
struct Params {
  const u16 *x, *lar, *aim, *cre, *cim, *ldt, *dsk, *w1, *w2;
  u16* out;
  char* ws;
};

DEV void ln_role(const Params& p, LAS char* sm, int tb) {
  const int tid = threadIdx.x, lane = tid & 63, w = __builtin_amdgcn_readfirstlane(tid >> 6);
  STAMP(p, 0);
  v4u xr[8];
#pragma unroll
  for (int r = 0; r < 8; ++r) xr[r] = *(const GAS v4u*)((const GAS char*)p.x + (size_t)(tb * 64 + w * 8 + r) * 1024 + lane * 16);
  unsigned o[8][4];
  float xn[8][8];
#pragma unroll
  for (int r = 0; r < 8; ++r) {
    float v[8];
#pragma unroll
    for (int i = 0; i < 4; ++i) { v[2 * i] = bflo(xr[r][i]); v[2 * i + 1] = bfhi(xr[r][i]); }
    float s = ((v[0] + v[1]) + (v[2] + v[3])) + ((v[4] + v[5]) + (v[6] + v[7]));
    const float mean = wave_sum(s) * (1.f / D);
    float q = 0.f;
#pragma unroll
    for (int i = 0; i < 8; ++i) { const float d = v[i] - mean; q = fmaf(d, d, q); }
    const float var = rbf(wave_sum(q) * (1.f / D));
    const float m = rbf(mean);
    const float sd = rbf(__builtin_sqrtf(rbf(var + 1e-5f)));
    const float rs = 1.f / sd;
#pragma unroll
    for (int i = 0; i < 4; ++i) {
      const unsigned a = pk_bf16(v[2 * i] - m, v[2 * i + 1] - m);
      const unsigned b = pk_bf16(bflo(a) * rs, bfhi(a) * rs);
      xn[r][2 * i] = bflo(b); xn[r][2 * i + 1] = bfhi(b);
    }
  }
#pragma unroll
  for (int j = 0; j < 8; ++j)
#pragma unroll
    for (int i = 0; i < 4; ++i) o[j][i] = pk_f16(xn[2 * i][j], xn[2 * i + 1][j]);
  STAMP(p, 1);
#pragma unroll
  for (int j = 0; j < 8; ++j)
    *(LAS v4u*)(sm + (8 * lane + j) * 128 + ((w ^ (lane & 7)) << 4)) = (v4u){o[j][0], o[j][1], o[j][2], o[j][3]};
  __syncthreads();
  const __amdgpu_buffer_rsrc_t rs = __builtin_amdgcn_make_buffer_rsrc(p.ws, 0, WS_BYTES, 0x00020000);
#pragma unroll
  for (int i = 0; i < 8; ++i) {
    const int e = i * 512 + tid;
    const int ch = e >> 3, c = e & 7;
    const v4u v = *(const LAS v4u*)(sm + ch * 128 + ((c ^ ((ch >> 3) & 7)) << 4));
    __builtin_amdgcn_raw_buffer_store_b128(v, rs, WS_XT + tb * 65536 + e * 16, 0, AUX_SC1);
  }
  STAMP(p, 2);
  asm volatile("s_waitcnt vmcnt(0)" ::: "memory");
  STAMP(p, 3);
}

constexpr int G1_SLOT = 32768;
constexpr int G1_RED = 4 * G1_SLOT;
DEV void glds16(const void* sbase, unsigned voff, unsigned lds_dst) {
  unsigned keep;
  asm volatile("s_mov_b32 %0, m0\n\ts_mov_b32 m0, %3\n\ts_nop 0\n\tglobal_load_lds_dwordx4 %1, %2\n\ts_mov_b32 m0, %0"
               : "=&s"(keep) : "v"(voff), "s"(sbase), "s"(lds_dst) : "memory");
}
DEV void g1_issue(const char* xb, const char* wb, unsigned lds0, int c, int slot, int w, const unsigned (&vo)[4]) {
  const unsigned base = lds0 + slot * G1_SLOT + w * 2048;
  glds16(xb + c * 128, vo[0], base);
  glds16(xb + c * 128, vo[1], base + 1024);
  glds16(wb + (size_t)c * 64 * (F * 2), vo[2], base + 16384);
  glds16(wb + (size_t)c * 64 * (F * 2), vo[3], base + 16384 + 1024);
}

DEV void gemm1_role(const Params& p, LAS char* sm, int rt, int ct) {
  const int tid = threadIdx.x, lane = tid & 63, w = __builtin_amdgcn_readfirstlane(tid >> 6);
  const int wf = w & 3, wt = w >> 2, h = lane >> 5, r32 = lane & 31;
  const char* xb = (const char*)p.x + (size_t)rt * 128 * 1024;
  const char* wb = (const char*)p.w1 + ct * 256;
  const unsigned lds0 = (unsigned)(size_t)sm;
  unsigned vo[4];
#pragma unroll
  for (int i = 0; i < 2; ++i) {
    const int rx = (w * 2 + i) * 8 + (lane >> 3), rw = (w * 2 + i) * 4 + (lane >> 4);
    vo[i] = rx * 1024 + (((lane & 7) ^ ((rx >> 1) & 7)) << 4);
    vo[2 + i] = rw * (F * 2) + (((lane & 15) ^ ((rw & 3) << 2)) << 4);
  }
  STAMP(p, 4);
  g1_issue(xb, wb, lds0, 0, 0, w, vo);
  g1_issue(xb, wb, lds0, 1, 1, w, vo);
  g1_issue(xb, wb, lds0, 2, 2, w, vo);
  f32x16 acc[2] = {};
  float sx[2] = {0.f, 0.f}, sq[2] = {0.f, 0.f}, cs = 0.f;
  const int tok0 = wt * 64 + r32;
  const int xsw = (tok0 >> 1) & 7;
  const int q4 = (lane & 15) >> 2, p4 = lane & 3, blk = (lane >> 4) & 1;
  int aoff[4], boff[4];
#pragma unroll
  for (int s = 0; s < 4; ++s) {
    const int se = (s + wf + 2 * wt) & 3;
    aoff[s] = 16384 + se * 4096 + (8 * h + q4) * 256 + (((4 * wf + 2 * blk + (p4 >> 1)) ^ (q4 << 2)) << 4) + 8 * (p4 & 1);
    boff[s] = tok0 * 128 + (((2 * se + h) ^ xsw) << 4);
  }
#pragma unroll
  for (int c = 0; c < 8; ++c) {
    if (c < 6) asm volatile("s_waitcnt vmcnt(8)" ::: "memory");
    else if (c == 6) asm volatile("s_waitcnt vmcnt(4)" ::: "memory");
    else asm volatile("s_waitcnt vmcnt(0)" ::: "memory");
    raw_barrier();
    if (c + 3 < 8) g1_issue(xb, wb, lds0, c + 3, (c + 3) & 3, w, vo);
    LAS char* base = sm + (c & 3) * G1_SLOT;
#pragma unroll
    for (int s = 0; s < 4; ++s) {
      const s16x4 a0 = __builtin_amdgcn_ds_read_tr16_b64_v4i16((LAS s16x4*)(base + aoff[s]));
      const s16x4 a1 = __builtin_amdgcn_ds_read_tr16_b64_v4i16((LAS s16x4*)(base + aoff[s] + 1024));
      const v2u a0u = __builtin_bit_cast(v2u, a0), a1u = __builtin_bit_cast(v2u, a1);
      const v4u a = {a0u.x, a0u.y, a1u.x, a1u.y};
      v4u b[2];
#pragma unroll
      for (int tt = 0; tt < 2; ++tt) b[tt] = *(const LAS v4u*)(base + boff[s] + 4096 * tt);
#pragma unroll
      for (int tt = 0; tt < 2; ++tt)
        acc[tt] = __builtin_amdgcn_mfma_f32_32x32x16_bf16(__builtin_bit_cast(bf16x8, a), __builtin_bit_cast(bf16x8, b[tt]), acc[tt], 0, 0, 0);
      if (s == 0) {
#pragma unroll
        for (int tt = 0; tt < 2; ++tt)
#pragma unroll
          for (int i = 0; i < 4; ++i) { sx[tt] = dot2bf(b[tt][i], 0x3f803f80u, sx[tt]); sq[tt] = dot2bf(b[tt][i], b[tt][i], sq[tt]); }
      }
      if (s < 2) {
#pragma unroll
        for (int i = 0; i < 4; ++i) cs = dot2bf(a[i], 0x3f803f80u, cs);
      }
    }
    asm volatile("" : "+v"(sx[0]), "+v"(sx[1]), "+v"(sq[0]), "+v"(sq[1]), "+v"(cs));
    __builtin_amdgcn_sched_barrier(0);
  }
  STAMP(p, 5);
  LAS float* red = (LAS float*)(sm + G1_RED);
#pragma unroll
  for (int tt = 0; tt < 2; ++tt) *(LAS f32x2*)(red + ((wf * 2 + h) * 128 + tok0 + 32 * tt) * 2) = (f32x2){sx[tt], sq[tt]};
  red[2048 + (wt * 2 + h) * 128 + wf * 32 + r32] = cs;
  raw_barrier();
  float mrs[2], rsv[2];
#pragma unroll
  for (int tt = 0; tt < 2; ++tt) {
    f32x2 a = {0.f, 0.f};
#pragma unroll
    for (int i = 0; i < 8; ++i) a += *(const LAS f32x2*)(red + (i * 128 + tok0 + 32 * tt) * 2);
    const float mean = a.x * (1.f / D);
    const float var = rbf(fmaxf(a.y * (1.f / D) - mean * mean, 0.f));
    const float sd = rbf(__builtin_sqrtf(rbf(var + 1e-5f)));
    rsv[tt] = 1.f / sd;
    mrs[tt] = rbf(mean);
  }
  float csv[16];
#pragma unroll
  for (int g = 0; g < 4; ++g) {
    f32x4 a = {0.f, 0.f, 0.f, 0.f};
#pragma unroll
    for (int i = 0; i < 4; ++i) a += *(const LAS f32x4*)(red + 2048 + i * 128 + wf * 32 + 8 * g + 4 * h);
#pragma unroll
    for (int i = 0; i < 4; ++i) csv[4 * g + i] = a[i];
  }
  const __amdgpu_buffer_rsrc_t rs = __builtin_amdgcn_make_buffer_rsrc(p.ws, 0, WS_BYTES, 0x00020000);
  const int hbase = WS_HF + ((2 * rt + wt) * 56 + 4 * ct + wf) * 4096 + (r32 >> 4) * 1024 + (h * 16 + (r32 & 15)) * 16;
#pragma unroll
  for (int tt = 0; tt < 2; ++tt)
#pragma unroll
    for (int q = 0; q < 2; ++q) {
      v4u o;
#pragma unroll
      for (int i = 0; i < 4; ++i) {
        const int r = 8 * q + 2 * i;
        const float v0 = (acc[tt][r] - mrs[tt] * csv[r]) * rsv[tt];
        const float v1 = (acc[tt][r + 1] - mrs[tt] * csv[r + 1]) * rsv[tt];
        o[i] = pk_bf16(silu_f(v0), silu_f(v1));
      }
      __builtin_amdgcn_raw_buffer_store_b128(o, rs, hbase + tt * 2048 + q * 512, 0, AUX_SC1);
    }
  STAMP(p, 6);
}

constexpr int KG_ROW = 528;
constexpr int KG_V = 0;
constexpr int KG_U = KG_V + 2 * 32 * KG_ROW;
constexpr int KG_T = KG_U + 2 * 32 * KG_ROW;
constexpr int KG_TCH = 2064;
constexpr int KG_M = KG_T + 2 * KG_TCH;
struct cplx { float re, im; };
DEV cplx cmul(cplx a, cplx b) { return {a.re * b.re - a.im * b.im, a.re * b.im + a.im * b.re}; }
DEV cplx cexp_at(float a2, float rv, float pos) {
  const float e = __builtin_amdgcn_exp2f(a2 * pos);
  const float ph = __builtin_amdgcn_fractf(rv * pos);
  return {e * __builtin_amdgcn_cosf(ph), e * __builtin_amdgcn_sinf(ph)};
}

DEV void kgen(const Params& p, LAS char* sm, int cp) {
  const int tid = threadIdx.x, lane = tid & 63, w = __builtin_amdgcn_readfirstlane(tid >> 6);
  const int role = w >> 2, chl = (w >> 1) & 1, n = (w & 1) * 64 + lane, d = 2 * cp + chl;
  STAMP(p, 7);
  const float dt = rbf(expf(bf2f(p.ldt[d])));
  const float ar = -rbf(expf(bf2f(p.lar[d * NM + n]))) * dt;
  const float ai = bf2f(p.aim[d * NM + n]) * dt;
  const float a2 = ar * 1.44269504088896f, rv = ai * 0.159154943091895f;
  if (role == 0) {
    LAS char* t = sm + KG_V + chl * 32 * KG_ROW + n * 4;
    const cplx w8 = cexp_at(a2, rv, 8.f);
    cplx v[4] = {{1.f, 0.f}, cexp_at(a2, rv, 64.f), cexp_at(a2, rv, 128.f), cexp_at(a2, rv, 192.f)};
#pragma unroll
    for (int i = 0; i < 8; ++i)
#pragma unroll
      for (int c = 0; c < 4; ++c) {
        *(LAS unsigned*)(t + (8 * c + i) * KG_ROW) = pk_f16(v[c].re, v[c].im);
        if (i < 7) v[c] = cmul(v[c], w8);
      }
  } else {
    const unsigned c0 = p.cre[d * NM + n], c1 = p.cim[d * NM + n], c2 = p.cre[d * NM + (n ^ 64)], c3 = p.cim[d * NM + (n ^ 64)];
    unsigned mx = max(max(c0 & 0x7fffu, c1 & 0x7fffu), max(c2 & 0x7fffu, c3 & 0x7fffu));
#pragma unroll
    for (int o = 1; o < 64; o <<= 1) mx = max(mx, (unsigned)__shfl_xor((int)mx, o));
    unsigned ex = mx >> 7;
    ex = min(max(ex, 1u), 253u);
    const float sc = __uint_as_float((254u - ex) << 23), isc = __uint_as_float(ex << 23);
    if ((w & 1) == 0 && lane == 0) *(LAS float*)(sm + KG_M + chl * 4) = 2.f * dt * isc;
    if ((w & 1) == 0 && lane < 2) *(LAS unsigned*)(sm + KG_T + chl * KG_TCH + lane * 4) = 0u;
    LAS char* t = sm + KG_U + chl * 32 * KG_ROW + n * 4;
    const cplx c = {bf2f(c0) * sc, bf2f(c1) * sc};
    const cplx w1 = cexp_at(a2, rv, 1.f), e256 = cexp_at(a2, rv, 256.f), e512 = cexp_at(a2, rv, 512.f), e1024 = cexp_at(a2, rv, 1024.f);
    const cplx w2 = cmul(w1, w1), w4 = cmul(w2, w2);
    auto put = [&](int row, cplx u) { *(LAS unsigned*)(t + row * KG_ROW) = pk_f16(u.re, -u.im); };
    cplx u = c, u4 = cmul(c, w4);
#pragma unroll
    for (int i = 0; i < 4; ++i) { put(i, u); put(4 + i, u4); if (i < 3) { u = cmul(u, w1); u4 = cmul(u4, w1); } }
    u = cmul(c, e256);
#pragma unroll
    for (int i = 0; i < 4; ++i) { put(8 + i, u); if (i < 3) u = cmul(u, w2); }
    const cplx u512 = cmul(c, e512), u768 = cmul(u512, e256), u1024 = cmul(c, e1024), u1536 = cmul(u1024, e512);
    put(12, u512); put(13, cmul(u512, w4)); put(14, u768); put(15, cmul(u768, w4));
    put(16, u1024); put(17, cmul(u1024, e256)); put(18, u1536); put(19, cmul(u1536, e256)); put(20, cmul(u1024, e1024));
  }
  STAMP(p, 8);
  raw_barrier();
  {
    const int ch = w >> 2, rh = (w >> 1) & 1, bh = w & 1, g = lane >> 4, r16 = lane & 15;
    const LAS char* ua = sm + KG_U + (ch * 32 + rh * 16 + r16) * KG_ROW + g * 16;
    const LAS char* va = sm + KG_V + (ch * 32 + bh * 16 + r16) * KG_ROW + g * 16;
    f32x4 acc = {0.f, 0.f, 0.f, 0.f};
#pragma unroll
    for (int s = 0; s < 8; ++s)
      acc = __builtin_amdgcn_mfma_f32_16x16x32_f16(*(const LAS f16x8*)(ua + s * 64), *(const LAS f16x8*)(va + s * 64), acc, 0, 0, 0);
    const int b = bh * 16 + r16;
    int base, s0, s1;
    if (rh == 0) { base = (g < 2) ? 4 * g + 8 * b : (g == 2 ? 256 + 4 * b : 384 + 2 * b); s0 = 1; s1 = (g == 3) ? 64 : 2; }
    else { base = 512 + 128 * g + b; s0 = 32; s1 = 64; }
    LAS _Float16* t = (LAS _Float16*)(sm + KG_T + ch * KG_TCH) + 4 + base;
    t[0] = (_Float16)acc[0]; t[s0] = (_Float16)acc[1]; t[s1] = (_Float16)acc[2]; t[s0 + s1] = (_Float16)acc[3];
  }
}

constexpr int CV_KS = 4288;
constexpr int CV_K = 0;
constexpr int CV_X = CV_K + 8 * CV_KS;
constexpr int CV_XS = (256 + 2048 + 16) * 2;
constexpr int CV_R = KG_M + 16;
static_assert(CV_X + 2 * CV_XS <= KG_T, "conv arrays must not overlap the compact table");
static_assert(CV_R % 16 == 0 && CV_R + 65536 <= LDS_BYTES, "lds");

DEV void conv_role(const Params& p, LAS char* sm, int cp) {
  const int tid = threadIdx.x, lane = tid & 63, w = __builtin_amdgcn_readfirstlane(tid >> 6);
  const __amdgpu_buffer_rsrc_t rs = __builtin_amdgcn_make_buffer_rsrc(p.ws, 0, WS_BYTES, 0x00020000);
  const v4u xv = __builtin_amdgcn_raw_buffer_load_b128(rs, WS_XT + (tid >> 4) * 65536 + cp * 256 + (tid & 15) * 16, 0, AUX_SC1);
  STAMP(p, 9);
  raw_barrier();
  {
    const int ch = tid >> 8, pp = tid & 255;
    const LAS u16* t = (const LAS u16*)(sm + KG_T + ch * KG_TCH) + 4;
    unsigned kv[11];
#pragma unroll
    for (int i = 0; i < 11; ++i) {
      const int lag = 8 * pp - 3 + i;
      const int code = (int)(pk_bf16((float)lag, 0.f) & 0xffffu) - 0x4280;
      kv[i] = t[lag < 128 ? lag : code];
    }
#pragma unroll
    for (int sg = 0; sg < 4; ++sg) {
      v4u o;
#pragma unroll
      for (int i = 0; i < 4; ++i) o[i] = kv[7 + sg - 2 * i] | (kv[6 + sg - 2 * i] << 16);
      *(LAS v4u*)(sm + CV_K + (ch * 4 + sg) * CV_KS + (516 - 2 * pp) * 8) = o;
    }
    if (tid < 128) {
      const int cpy = tid >> 4, i = tid & 15;
      *(LAS v2u*)(sm + CV_K + cpy * CV_KS + (i < 6 ? i : 512 + i) * 8) = (v2u){0u, 0u};
    }
    if (tid < 68) {
      const int ch2 = tid / 34, i = tid % 34;
      *(LAS v4u*)(sm + CV_X + ch2 * CV_XS + (i < 32 ? i : 256 + i) * 16) = (v4u){0u, 0u, 0u, 0u};
    }
    *(LAS v4u*)(sm + CV_X + ((tid >> 3) & 1) * CV_XS + 512 + (tid >> 4) * 128 + (tid & 7) * 16) = xv;
  }
  STAMP(p, 10);
  raw_barrier();
  STAMP(p, 11);
  const int ch = w >> 2, q = w & 3, g = lane >> 4, r16 = lane & 15;
  const LAS char* ka = sm + CV_K + (ch * 4 + (r16 & 3)) * CV_KS + (517 - (r16 >> 2) + 2 * g) * 8;
  const LAS char* xa = sm + CV_X + ch * CV_XS + 512 + r16 * 32 + g * 16;
  f32x4 acc[8];
#pragma unroll
  for (int a = 0; a < 8; ++a) acc[a] = (f32x4){0.f, 0.f, 0.f, 0.f};
  auto lda = [&](const LAS char* ptr) {
    const v2u lo = *(const volatile LAS v2u*)ptr, hi = *(const volatile LAS v2u*)(ptr + 8);
    return __builtin_bit_cast(f16x8, (v4u){lo.x, lo.y, hi.x, hi.y});
  };
#pragma unroll 1
  for (int e = 0; e < 2; ++e) {
    const int E2 = 64 * (2 * q + 1 + e);
    f16x8 A[8], B[8];
#pragma unroll
    for (int i = 0; i < 8; ++i) { A[i] = lda(ka - E2 - 512 * i); B[i] = *(const LAS f16x8*)(xa - E2 + 512 * i); }
#pragma unroll
    for (int dd = 0; dd < 8; ++dd)
#pragma unroll
      for (int a = dd; a < 8; ++a) acc[a] = __builtin_amdgcn_mfma_f32_16x16x32_f16(A[dd], B[a - dd], acc[a], 0, 0, 0);
  }
  STAMP(p, 12);
  LAS f32x4* red = (LAS f32x4*)(sm + CV_R);
#pragma unroll
  for (int a = 0; a < 8; ++a) red[(w * 8 + a) * 64 + lane] = acc[a];
  f32x4 fin[2];
  {
    const f16x8 A0 = lda(ka);
#pragma unroll
    for (int i = 0; i < 2; ++i)
      fin[i] = __builtin_amdgcn_mfma_f32_16x16x32_f16(A0, *(const LAS f16x8*)(xa + 512 * (2 * q + i)), (f32x4){0.f, 0.f, 0.f, 0.f}, 0, 0, 0);
  }
  raw_barrier();
  STAMP(p, 13);
  const float mult = *(const LAS float*)(sm + KG_M + ch * 4);
  const float dsk = bf2f(p.dsk[2 * cp + ch]);
#pragma unroll
  for (int i = 0; i < 2; ++i) {
    const int a = 2 * q + i;
    f32x4 y = fin[i];
#pragma unroll
    for (int ww = 0; ww < 4; ++ww) y += red[((ch * 4 + ww) * 8 + a) * 64 + lane];
    const int t0 = 256 * a + 16 * r16 + 4 * g;
    const v2u xr = *(const LAS v2u*)(sm + CV_X + ch * CV_XS + 512 + t0 * 2);
    const unsigned xr0 = xr.x, xr1 = xr.y;
    const f16x2 x0 = __builtin_bit_cast(f16x2, xr0), x1 = __builtin_bit_cast(f16x2, xr1);
    const unsigned y01 = pk_bf16(y[0] * mult, y[1] * mult), y23 = pk_bf16(y[2] * mult, y[3] * mult);
    const unsigned d01 = pk_bf16((float)x0.x * dsk, (float)x0.y * dsk), d23 = pk_bf16((float)x1.x * dsk, (float)x1.y * dsk);
    v2u o;
    o.x = pk_bf16(bflo(y01) + bflo(d01), bfhi(y01) + bfhi(d01));
    o.y = pk_bf16(bflo(y23) + bflo(d23), bfhi(y23) + bfhi(d23));
    __builtin_amdgcn_raw_buffer_store_b64(o, rs, WS_ST + (2 * cp + ch) * 4096 + t0 * 2, 0, AUX_SC1);
  }
  STAMP(p, 14);
  asm volatile("s_waitcnt vmcnt(0)" ::: "memory");
  STAMP(p, 15);
}

DEV void gemm2_role(const Params& p, LAS char* sm, int rt, int ct) {
  const int tid = threadIdx.x, lane = tid & 63, w = __builtin_amdgcn_readfirstlane(tid >> 6);
  const int g = lane >> 4, hb = (lane >> 3) & 1, cb = lane & 7;
  const __amdgpu_buffer_rsrc_t rs = __builtin_amdgcn_make_buffer_rsrc(p.ws, 0, WS_BYTES, 0x00020000);
  STAMP(p, 16);
  v4u wr[28], ha[28];
  const GAS char* wb = (const GAS char*)p.w2 + (size_t)(w * 224 + 16 * (g >> 1) + 8 * hb + 4 * (g & 1)) * 1024 + ct * 128 + cb * 16;
#pragma unroll
  for (int i = 0; i < 28; ++i) wr[i] = *(const GAS v4u*)(wb + ((i >> 2) * 32 + (i & 3)) * 1024);
  const int hoff = WS_HF + (rt * 56 + w * 7) * 4096 + lane * 16;
#pragma unroll
  for (int i = 0; i < 12; ++i) ha[i] = __builtin_amdgcn_raw_buffer_load_b128(rs, hoff + i * 1024, 0, AUX_SC1);
  __builtin_amdgcn_sched_barrier(0);
  f32x4 acc[4][4];
#pragma unroll
  for (int i = 0; i < 4; ++i)
#pragma unroll
    for (int j = 0; j < 4; ++j) acc[i][j] = (f32x4){0.f, 0.f, 0.f, 0.f};
#pragma unroll
  for (int ks = 0; ks < 7; ++ks) {
    unsigned P[4][2], Q[4][2];
#pragma unroll
    for (int i = 0; i < 4; ++i)
#pragma unroll
      for (int u = 0; u < 2; ++u) {
        const unsigned X = wr[4 * ks + i][u], Y = wr[4 * ks + i][2 + u];
        P[i][u] = __builtin_amdgcn_update_dpp(X, Y, 0x128, 0xf, 0xc, false);
        Q[i][u] = __builtin_amdgcn_update_dpp(Y, X, 0x128, 0xf, 0x3, false);
      }
    v4u B[4];
#pragma unroll
    for (int t = 0; t < 4; ++t) {
      const unsigned sel = (t & 1) ? 0x07060302u : 0x05040100u;
      const int u = t >> 1;
      B[t] = (v4u){__builtin_amdgcn_perm(P[1][u], P[0][u], sel), __builtin_amdgcn_perm(P[3][u], P[2][u], sel),
                   __builtin_amdgcn_perm(Q[1][u], Q[0][u], sel), __builtin_amdgcn_perm(Q[3][u], Q[2][u], sel)};
    }
    if (ks + 3 < 7) {
#pragma unroll
      for (int i = 0; i < 4; ++i) ha[4 * (ks + 3) + i] = __builtin_amdgcn_raw_buffer_load_b128(rs, hoff + (4 * (ks + 3) + i) * 1024, 0, AUX_SC1);
    }
#pragma unroll
    for (int mt = 0; mt < 4; ++mt)
#pragma unroll
      for (int t = 0; t < 4; ++t)
        acc[mt][t] = __builtin_amdgcn_mfma_f32_16x16x32_bf16(__builtin_bit_cast(bf16x8, ha[4 * ks + mt]), __builtin_bit_cast(bf16x8, B[t]), acc[mt][t], 0, 0, 0);
    __builtin_amdgcn_sched_barrier(0);
  }
  STAMP(p, 17);
  LAS f32x4* red = (LAS f32x4*)sm;
#pragma unroll
  for (int mt = 0; mt < 4; ++mt)
#pragma unroll
    for (int t = 0; t < 4; ++t) red[(w * 16 + mt * 4 + t) * 64 + lane] = acc[mt][t];
  const int mt = w >> 1, tp = w & 1;
  const int tok = rt * 64 + 16 * mt + 4 * g, chn = ct * 64 + 8 * cb + 4 * hb + 2 * tp;
  v2u sv[2];
#pragma unroll
  for (int u = 0; u < 2; ++u) sv[u] = __builtin_amdgcn_raw_buffer_load_b64(rs, WS_ST + (chn + u) * 4096 + tok * 2, 0, AUX_SC1);
  unsigned xv[4];
#pragma unroll
  for (int i = 0; i < 4; ++i) xv[i] = *(const GAS unsigned*)((const GAS char*)p.x + (size_t)(tok + i) * 1024 + chn * 2);
  raw_barrier();
  STAMP(p, 18);
  f32x4 o[2];
#pragma unroll
  for (int u = 0; u < 2; ++u) {
    o[u] = red[(mt * 4 + 2 * tp + u) * 64 + lane];
#pragma unroll
    for (int ww = 1; ww < 8; ++ww) o[u] += red[(ww * 16 + mt * 4 + 2 * tp + u) * 64 + lane];
  }
#pragma unroll
  for (int i = 0; i < 4; ++i) {
    const unsigned ob = pk_bf16(o[0][i], o[1][i]);
    const unsigned gb = pk_bf16(silu_f(bflo(ob)), silu_f(bfhi(ob)));
    const unsigned s0 = sv[0][i >> 1], s1 = sv[1][i >> 1];
    const float sa = (i & 1) ? bfhi(s0) : bflo(s0), sb = (i & 1) ? bfhi(s1) : bflo(s1);
    const unsigned pr = pk_bf16(sa * bflo(gb), sb * bfhi(gb));
    *(GAS unsigned*)((GAS char*)p.out + (size_t)(tok + i) * 1024 + chn * 2) = pk_bf16(bflo(xv[i]) + bflo(pr), bfhi(xv[i]) + bfhi(pr));
  }
  STAMP(p, 19);
}

__global__ void __launch_bounds__(512, 2) k_pre(Params p) {
  __shared__ __attribute__((aligned(16))) char smem[65536];
  ln_role(p, (LAS char*)smem, blockIdx.x);
}
__global__ void __launch_bounds__(512, 2) k_mid(Params p) {
  __shared__ __attribute__((aligned(16))) char smem[LDS_BYTES];
  const int b = blockIdx.x, xcd = b & 7, idx = b >> 3;
  if (idx >= 4) {
    const int i = idx - 4;
    gemm1_role(p, (LAS char*)smem, (xcd & 3) * 4 + (i & 3), (xcd >> 2) * 7 + (i >> 2));
    raw_barrier();
  }
  kgen(p, (LAS char*)smem, b);
  conv_role(p, (LAS char*)smem, b);
}
__global__ void __launch_bounds__(512, 2) k_out(Params p) {
  __shared__ __attribute__((aligned(16))) char smem[131072];
  const int b = blockIdx.x, xcd = b & 7, idx = b >> 3;
  gemm2_role(p, (LAS char*)smem, xcd * 4 + (idx & 3), idx >> 2);
}

extern "C" void kernel_launch(void* const* d_in, const int* in_sizes, int n_in, void* d_out, int out_size, void* d_ws, size_t ws_size,
                              hipStream_t stream) {
  Params p;
  p.x = (const u16*)d_in[0]; p.lar = (const u16*)d_in[1]; p.aim = (const u16*)d_in[2]; p.cre = (const u16*)d_in[3];
  p.cim = (const u16*)d_in[4]; p.ldt = (const u16*)d_in[5]; p.dsk = (const u16*)d_in[6]; p.w1 = (const u16*)d_in[8];
  p.w2 = (const u16*)d_in[9]; p.out = (u16*)d_out; p.ws = (char*)d_ws;
  k_pre<<<32, 512, 0, stream>>>(p);
  k_mid<<<256, 512, 0, stream>>>(p);
  k_out<<<256, 512, 0, stream>>>(p);
}
```

```cpp
#include <hip/hip_runtime.h>
#include <hip/hip_bf16.h>
#include <stdint.h>

typedef unsigned short u16;
typedef unsigned long long u64;
typedef unsigned v4u __attribute__((ext_vector_type(4)));
typedef unsigned v2u __attribute__((ext_vector_type(2)));
typedef float f32x2 __attribute__((ext_vector_type(2)));
typedef float f32x4 __attribute__((ext_vector_type(4)));
typedef float f32x16 __attribute__((ext_vector_type(16)));
typedef short s16x4 __attribute__((ext_vector_type(4)));
typedef __bf16 bf16x2 __attribute__((ext_vector_type(2)));
typedef __bf16 bf16x8 __attribute__((ext_vector_type(8)));
typedef _Float16 f16x2 __attribute__((ext_vector_type(2)));
typedef _Float16 f16x8 __attribute__((ext_vector_type(8)));
typedef __amdgpu_buffer_rsrc_t rsrc_t;
#define GAS __attribute__((address_space(1)))
#define LAS __attribute__((address_space(3)))
#define DEV __device__ __forceinline__

constexpr int L = 2048, D = 512, NM = 128, F = 1792;
constexpr int WS_EPOCH = 0;
constexpr int FSTR = 64;
constexpr int WS_FH = 4096;
constexpr int WS_FX = WS_FH + 256 * FSTR;
constexpr int WS_FW = WS_FX + 256 * FSTR;
constexpr int WS_FS = WS_FW + 256 * FSTR;
constexpr int WS_GS = WS_FS + 256 * FSTR;
constexpr int WS_GC = WS_GS + L * 8;
constexpr int WS_XT = 1 << 17;
constexpr int WS_ST = WS_XT + (2 << 20);
constexpr int WS_W2 = WS_ST + (2 << 20);
constexpr int WS_HF = WS_W2 + (2 << 20);
constexpr int WS_BYTES = WS_HF + L * F * 2;
#define LOADER_PRIO 0
#define LOADER_DELAY 10
constexpr int SC1 = 16;
constexpr int W2_EARLY = 10;
constexpr int CODE_BYTES = 27 * 1024;
constexpr unsigned SPIN_CAP = 1u << 16;
constexpr int G1_SLOT = 32768, G1_R = 3;
constexpr int KG_ROW = 272;
constexpr int KG_WAVE = 53 * KG_ROW;
constexpr int KG_TAB = G1_R * G1_SLOT;
constexpr int CV_KS = 4288;
constexpr int KG_K = KG_TAB + 4 * KG_WAVE;
constexpr int KG_KCH = 676 * 4;
constexpr int SY = KG_K + 2 * KG_KCH;
constexpr int GR = SY + 64;
constexpr int LDS_USED = GR + 1024;
constexpr int G2_S = 144384;
constexpr int CV_X = 0;
constexpr int CV_XS = (256 + 2048 + 16) * 2;
constexpr int CV_E = 2 * CV_XS;
constexpr int LDS_BYTES = 160 * 1024;
static_assert(WS_GC + F * 8 <= WS_XT && G2_S >= KG_TAB + 2 * KG_WAVE + 4 * CV_KS && G2_S + 64 * 144 <= KG_K, "maps");
static_assert(4 * CV_KS <= 2 * KG_WAVE && (2 * KG_WAVE) % 16 == 0 && LDS_USED <= LDS_BYTES && CV_E % 16 == 0 && CV_E + 16384 <= G1_SLOT, "lds map");

DEV float bf2f(unsigned v) { return __uint_as_float(v << 16); }
DEV float bflo(unsigned v) { return __uint_as_float(v << 16); }
DEV float bfhi(unsigned v) { return __uint_as_float(v & 0xffff0000u); }
DEV unsigned pk_bf16(float a, float b) { f32x2 f = {a, b}; return __builtin_bit_cast(unsigned, __builtin_convertvector(f, bf16x2)); }
DEV unsigned pk_f16(float a, float b) { f32x2 f = {a, b}; return __builtin_bit_cast(unsigned, __builtin_convertvector(f, f16x2)); }
DEV float rbf(float a) { return bflo(pk_bf16(a, a) & 0xffffu); }
DEV float dot2bf(unsigned a, unsigned b, float c) {
  return __builtin_amdgcn_fdot2_f32_bf16(__builtin_bit_cast(bf16x2, a), __builtin_bit_cast(bf16x2, b), c, false);
}
DEV float silu_f(float v) { return v * __builtin_amdgcn_rcpf(1.f + __builtin_amdgcn_exp2f(v * -1.44269504088896f)); }
DEV void lds_barrier() { asm volatile("s_waitcnt lgkmcnt(0)" ::: "memory"); __builtin_amdgcn_s_barrier(); asm volatile("" ::: "memory"); }
DEV void drain() { asm volatile("s_waitcnt vmcnt(0)" ::: "memory"); }
template <int CTRL> DEV float dpp_add(float v) {
  return v + __builtin_bit_cast(float, __builtin_amdgcn_update_dpp(0, __builtin_bit_cast(int, v), CTRL, 0xf, 0xf, true));
}
DEV float row16_sum(float v) { v = dpp_add<0xb1>(v); v = dpp_add<0x4e>(v); v = dpp_add<0x124>(v); return dpp_add<0x128>(v); }
DEV float swap32_add(float a, float b) {
  auto r = __builtin_amdgcn_permlane32_swap(__float_as_uint(a), __float_as_uint(b), false, false);
  return __uint_as_float(r[0]) + __uint_as_float(r[1]);
}
DEV float swap16_add(float a, float b) {
  auto r = __builtin_amdgcn_permlane16_swap(__float_as_uint(a), __float_as_uint(b), false, false);
  return __uint_as_float(r[0]) + __uint_as_float(r[1]);
}
DEV float rdlane(float v, int l) { return __uint_as_float(__builtin_amdgcn_readlane(__float_as_uint(v), l)); }
template <int I> struct IC { static constexpr int v = I; };
template <int B, int E, class Fn> DEV void static_for(Fn&& f) { if constexpr (B < E) { f(IC<B>{}); static_for<B + 1, E>(f); } }

struct Ts {};
#define STAMP(p, i)

struct Params {
  const u16 *x, *lar, *aim, *cre, *cim, *ldt, *dsk, *w1, *w2;
  u16* out;
  char* ws;
};

DEV void wait_flags(rsrc_t rs, int off, int n, v2u tag, int lane) {
#pragma unroll 1
  for (unsigned spins = 0; spins < SPIN_CAP; ++spins) {
    asm volatile("" ::: "memory");
    const v2u v = __builtin_amdgcn_raw_buffer_load_b64(rs, off + min(lane, n - 1) * FSTR, 0, SC1);
    if (__all(v.x == tag.x && v.y == tag.y)) break;
    __builtin_amdgcn_s_sleep(4);
  }
  __builtin_amdgcn_fence(__ATOMIC_ACQUIRE, "wavefront");
}
DEV void publish(rsrc_t rs, LAS char* sm, int word, unsigned nwaves, int off, v2u tag, int lane) {
  drain();
  if (lane == 0) {
    const unsigned old = __hip_atomic_fetch_add((LAS unsigned*)(sm + SY) + word, 1u, __ATOMIC_RELAXED, __HIP_MEMORY_SCOPE_WORKGROUP);
    if (old == nwaves - 1) __builtin_amdgcn_raw_buffer_store_b64(tag, rs, off, 0, SC1);
  }
}
DEV void wave_sync(LAS char* sm, int word, unsigned n) {
  asm volatile("s_waitcnt lgkmcnt(0)" ::: "memory");
  volatile LAS unsigned* c = (volatile LAS unsigned*)(sm + SY) + word;
  if ((threadIdx.x & 63) == 0) __hip_atomic_fetch_add((LAS unsigned*)c, 1u, __ATOMIC_RELAXED, __HIP_MEMORY_SCOPE_WORKGROUP);
#pragma unroll 1
  for (unsigned spins = 0; spins < 16 * SPIN_CAP && __builtin_amdgcn_readfirstlane(*c) != n; ++spins) {}
  asm volatile("" ::: "memory");
}

DEV void lds_set(LAS char* sm, int word) { *((volatile LAS unsigned*)(sm + SY) + word) = 1u; }
DEV void lds_wait(LAS char* sm, int word, unsigned want = 1u) {
  volatile LAS unsigned* c = (volatile LAS unsigned*)(sm + SY) + word;
#pragma unroll 1
  for (unsigned spins = 0; spins < 16 * SPIN_CAP && __builtin_amdgcn_readfirstlane(*c) != want; ++spins) __builtin_amdgcn_s_sleep(1);
  __builtin_amdgcn_fence(__ATOMIC_ACQUIRE, "wavefront");
  asm volatile("" ::: "memory");
}
DEV void bar() { asm volatile("" ::: "memory"); __builtin_amdgcn_s_barrier(); asm volatile("" ::: "memory"); }

DEV void ln_wave(const Params& p, Ts& ts, rsrc_t rs, int t8, int lane, v2u tag, const v4u (&xr)[8]) {
  float s[8], q[8], t[4], rz[2], mz[2];
#pragma unroll
  for (int r = 0; r < 8; ++r) {
    s[r] = 0.f; q[r] = 0.f;
#pragma unroll
    for (int j = 0; j < 4; ++j) { s[r] = dot2bf(xr[r][j], 0x3f803f80u, s[r]); q[r] = dot2bf(xr[r][j], xr[r][j], q[r]); }
  }
#pragma unroll
  for (int j = 0; j < 4; ++j) { t[j] = swap32_add(s[j], s[j + 4]); q[j] = swap32_add(q[j], q[j + 4]); }
#pragma unroll
  for (int u = 0; u < 2; ++u) {
    const float mean = row16_sum(swap16_add(t[u], t[u + 2])) * (1.f / D);
    const float var = rbf(fmaxf(row16_sum(swap16_add(q[u], q[u + 2])) * (1.f / D) - mean * mean, 0.f));
    const unsigned ms = pk_bf16(mean, __builtin_amdgcn_sqrtf(rbf(var + 1e-5f)));
    if ((lane & 15) == 0) __builtin_amdgcn_raw_buffer_store_b64((v2u){ms, tag.x}, rs, WS_GS + (t8 * 8 + u + 2 * (lane >> 4)) * 8, 0, SC1);
    rz[u] = __builtin_amdgcn_rcpf(bfhi(ms));
    mz[u] = -bflo(ms) * rz[u];
  }
  float rr[8], mm[8];
#pragma unroll
  for (int r = 0; r < 8; ++r) { rr[r] = rdlane(rz[r & 1], 16 * (r >> 1)); mm[r] = rdlane(mz[r & 1], 16 * (r >> 1)); }
  STAMP(p, 21);
#pragma unroll
  for (int j = 0; j < 4; ++j) {
    __builtin_amdgcn_sched_barrier(0);
    f32x2 v[8];
#pragma unroll
    for (int r = 0; r < 8; ++r) v[r] = __builtin_elementwise_fma((f32x2){bflo(xr[r][j]), bfhi(xr[r][j])}, (f32x2){rr[r], rr[r]}, (f32x2){mm[r], mm[r]});
#pragma unroll
    for (int e = 0; e < 2; ++e) {
      v4u o;
#pragma unroll
      for (int i = 0; i < 4; ++i) o[i] = pk_f16(v[2 * i][e], v[2 * i + 1][e]);
      __builtin_amdgcn_raw_buffer_store_b128(o, rs, WS_XT + t8 * 8192 + (2 * j + e) * 1024 + lane * 16, 0, SC1);
    }
  }
}

DEV void cs_load(const Params& p, int cb, int w, int lane, v4u (&wr)[16]) {
#pragma unroll
  for (int i = 0; i < 16; ++i) wr[i] = *(const GAS v4u*)((const GAS char*)p.w1 + (size_t)(w * 128 + i * 8 + (lane >> 3)) * (F * 2) + cb * 128 + (lane & 7) * 16);
}
DEV void cs_role(LAS char* sm, rsrc_t rs, int tb, bool act, int w, int lane, v2u tag, const v4u (&wr)[16]) {
  unsigned sel_lo = 0x00003f80u, sel_hi = 0x3f800000u;
  asm volatile("" : "+s"(sel_lo), "+s"(sel_hi));
  float c[8] = {0.f, 0.f, 0.f, 0.f, 0.f, 0.f, 0.f, 0.f}, t[4], y[2];
#pragma unroll
  for (int i = 0; i < 16; ++i)
#pragma unroll
    for (int j = 0; j < 4; ++j) { c[2 * j] = dot2bf(sel_lo, wr[i][j], c[2 * j]); c[2 * j + 1] = dot2bf(sel_hi, wr[i][j], c[2 * j + 1]); }
#pragma unroll
  for (int j = 0; j < 4; ++j) t[j] = swap32_add(c[j], c[j + 4]);
#pragma unroll
  for (int u = 0; u < 2; ++u) y[u] = dpp_add<0x128>(swap16_add(t[u], t[u + 2]));
  if ((lane & 8) == 0) *(LAS f32x2*)(sm + 65536 + (w * 64 + (lane & 7) * 8 + 2 * (lane >> 4)) * 4) = (f32x2){y[0], y[1]};
  lds_barrier();
  if (w == 0 && act) {
    float cs = 0.f;
#pragma unroll
    for (int ww = 0; ww < 4; ++ww) cs += *(const LAS float*)(sm + 65536 + (ww * 64 + lane) * 4);
    const float hi = rbf(cs);
    __builtin_amdgcn_raw_buffer_store_b64((v2u){pk_bf16(hi, cs - hi), tag.x}, rs, WS_GC + (tb * 64 + lane) * 8, 0, SC1);
  }
}

DEV void w2_piece_store(rsrc_t rs, int piece, int lane, const v4u* wr) {
  unsigned P[4][2], Q[4][2];
#pragma unroll
  for (int i = 0; i < 4; ++i)
#pragma unroll
    for (int u = 0; u < 2; ++u) {
      const unsigned X = wr[i][u], Y = wr[i][2 + u];
      P[i][u] = __builtin_amdgcn_update_dpp(X, Y, 0x128, 0xf, 0xc, false);
      Q[i][u] = __builtin_amdgcn_update_dpp(Y, X, 0x128, 0xf, 0x3, false);
    }
#pragma unroll
  for (int t = 0; t < 4; ++t) {
    const unsigned sel = (t & 1) ? 0x07060302u : 0x05040100u;
    const int u = t >> 1;
    const v4u B = {__builtin_amdgcn_perm(P[1][u], P[0][u], sel), __builtin_amdgcn_perm(P[3][u], P[2][u], sel),
                   __builtin_amdgcn_perm(Q[1][u], Q[0][u], sel), __builtin_amdgcn_perm(Q[3][u], Q[2][u], sel)};
    __builtin_amdgcn_raw_buffer_store_b128(B, rs, WS_W2 + piece * 4096 + t * 1024 + lane * 16, 0, SC1);
  }
}

struct KPar { unsigned ldt, lar, aim, c0, c1, c2, c3, dsk; };
DEV int chan_of(int b, int ch) { return 16 * (b >> 3) + 8 * ch + (b & 7); }
DEV f32x2 crot(f32x2 w) { return (f32x2){-w.y, w.x}; }
DEV f32x2 cmul(f32x2 a, f32x2 w, f32x2 wr) { return __builtin_elementwise_fma(a.yy, wr, a.xx * w); }
DEV f32x2 cexp_at(float a2, float rv, float pos) {
  const float e = __builtin_amdgcn_exp2f(a2 * pos);
  const float ph = __builtin_amdgcn_fractf(rv * pos);
  return (f32x2){__builtin_amdgcn_cosf(ph), __builtin_amdgcn_sinf(ph)} * e;
}
DEV float exp_acc(float x) {
  const float t = x * 1.44269504088896f;
  const float r = fmaf(x, 1.92596299112661e-8f, fmaf(x, 1.44269504088896f, -t));
  const float e = __builtin_amdgcn_exp2f(t);
  return fmaf(e, r * 0.693147180559945f, e);
}
DEV void kgen(const Params& p, Ts& ts, LAS char* sm, const KPar& k, int w, int lane) {
  const int ch = w >> 1;
  const float dt = rbf(exp_acc(bf2f(k.ldt)));
  const float a2 = -rbf(exp_acc(bf2f(k.lar))) * dt * 1.44269504088896f;
  const float rv = bf2f(k.aim) * dt * 0.159154943091895f;
  LAS char* t = sm + KG_TAB + w * KG_WAVE + lane * 4;
  auto put = [&](int row, f32x2 u) { *(LAS unsigned*)(t + row * KG_ROW) = pk_f16(u.x, u.y); };
  {
    const f32x2 w8 = cexp_at(a2, -rv, 8.f), w8r = crot(w8);
    const f32x2 w64 = cexp_at(a2, -rv, 64.f), w64r = crot(w64), w128 = cmul(w64, w64, w64r);
    f32x2 v[4] = {{1.f, 0.f}, w64, w128, cmul(w128, w64, w64r)};
#pragma unroll
    for (int i = 0; i < 8; ++i)
#pragma unroll
      for (int c = 0; c < 4; ++c) { put(21 + 8 * c + i, v[c]); if (i < 7) v[c] = cmul(v[c], w8, w8r); }
  }
  {
    unsigned mx = max(max(k.c0 & 0x7fffu, k.c1 & 0x7fffu), max(k.c2 & 0x7fffu, k.c3 & 0x7fffu));
    mx = max(mx, (unsigned)__builtin_amdgcn_update_dpp(0, (int)mx, 0xb1, 0xf, 0xf, true));
    mx = max(mx, (unsigned)__builtin_amdgcn_update_dpp(0, (int)mx, 0x4e, 0xf, 0xf, true));
    mx = max(mx, (unsigned)__builtin_amdgcn_update_dpp(0, (int)mx, 0x124, 0xf, 0xf, true));
    mx = max(mx, (unsigned)__builtin_amdgcn_update_dpp(0, (int)mx, 0x128, 0xf, 0xf, true));
    unsigned ex = max(max(__builtin_amdgcn_readlane(mx, 0), __builtin_amdgcn_readlane(mx, 16)), max(__builtin_amdgcn_readlane(mx, 32), __builtin_amdgcn_readlane(mx, 48))) >> 7;
    ex = min(max(ex, 1u), 253u);
    const float sc = __uint_as_float((254u - ex) << 23), isc = __uint_as_float(ex << 23);
    if (lane == 0) *(LAS f32x2*)(sm + SY + 48 + ch * 8) = (f32x2){2.f * dt * isc, bf2f(k.dsk)};
    const f32x2 c = (f32x2){bf2f(k.c0), bf2f(k.c1)} * sc;
    const f32x2 w1 = cexp_at(a2, rv, 1.f), e256 = cexp_at(a2, rv, 256.f), e256r = crot(e256), e512 = cmul(e256, e256, e256r), e1024 = cmul(e512, e512, crot(e512));
    const f32x2 w1r = crot(w1), w2 = cmul(w1, w1, w1r), w2r = crot(w2), w4 = cmul(w2, w2, w2r), w4r = crot(w4), cr = crot(c);
    f32x2 u = c, u4 = cmul(c, w4, w4r), u2 = cmul(e256, c, cr);
#pragma unroll
    for (int i = 0; i < 4; ++i) {
      put(i, u); put(4 + i, u4); put(8 + i, u2);
      if (i < 3) { u = cmul(u, w1, w1r); u4 = cmul(u4, w1, w1r); u2 = cmul(u2, w2, w2r); }
    }
    const f32x2 u512 = cmul(e512, c, cr), u768 = cmul(u512, e256, e256r), u1024 = cmul(e1024, c, cr), u1024r = crot(u1024);
    const f32x2 u1536 = cmul(e512, u1024, u1024r);
    put(12, u512); put(13, cmul(u512, w4, w4r)); put(14, u768); put(15, cmul(u768, w4, w4r));
    put(16, u1024); put(17, cmul(u1024, e256, e256r)); put(18, u1536); put(19, cmul(u1536, e256, e256r)); put(20, cmul(e1024, u1024, u1024r));
  }
  asm volatile("" ::: "memory");
  STAMP(p, 17);
  {
    const int h = lane >> 5, b = lane & 31;
    const LAS char* ua = sm + KG_TAB + w * KG_WAVE + b * KG_ROW + h * 16;
    f32x16 acc = {};
    f16x8 fa[8], fb[8];
#pragma unroll
    for (int s = 0; s < 8; ++s) { fa[s] = *(const LAS f16x8*)(ua + s * 32); fb[s] = *(const LAS f16x8*)(ua + 21 * KG_ROW + s * 32); }
    __builtin_amdgcn_sched_barrier(0);
#pragma unroll
    for (int s = 0; s < 8; ++s) acc = __builtin_amdgcn_mfma_f32_32x32x16_f16(fa[s], fb[s], acc, 0, 0, 0);
    STAMP(p, 18);
    LAS _Float16* kt = (LAS _Float16*)(sm + KG_K + ch * KG_KCH) + 8 + (w & 1);
    auto add = [&](int i, float v) { kt[2 * i] = (_Float16)v; };
    const int i0 = 4 * h + 8 * b, i1 = h ? 384 + 2 * b : 256 + 4 * b, i1s = h ? 64 : 2;
#pragma unroll
    for (int r = 0; r < 4; ++r) add(i0 + r, acc[r]);
    add(i1, acc[4]); add(i1 + 1, acc[5]); add(i1 + i1s, acc[6]); add(i1 + i1s + 1, acc[7]);
    add(512 + 128 * h + b, acc[8]);
    if (h == 0) { add(544 + b, acc[9]); add(576 + b, acc[10]); add(608 + b, acc[11]); }
  }
}
DEV void kexpand(LAS char* sm, int hw, int lane) {
  const int ch = hw >> 1;
  const LAS unsigned* kt = (const LAS unsigned*)(sm + KG_K + ch * KG_KCH) + 4;
  LAS char* kc = sm + KG_TAB + ch * 2 * KG_WAVE;
  auto sum2 = [&](unsigned hi, unsigned lo) {
    const unsigned a = __builtin_amdgcn_perm(hi, lo, 0x05040100u), b = __builtin_amdgcn_perm(hi, lo, 0x07060302u);
    return __builtin_bit_cast(unsigned, __builtin_bit_cast(f16x2, a) + __builtin_bit_cast(f16x2, b));
  };
  auto emit = [&](int pp, const unsigned (&pr)[10]) {
#pragma unroll
    for (int sg = 0; sg < 4; ++sg) *(LAS v4u*)(kc + sg * CV_KS + (516 - 2 * pp) * 8) = (v4u){pr[6 + sg], pr[4 + sg], pr[2 + sg], pr[sg]};
  };
  {
    const int pp = (hw & 1) * 64 + lane;
    unsigned kv[12], pr[10], ev[6];
#pragma unroll
    for (int i = 0; i < 11; ++i) {
      const int lag = 8 * pp - 3 + i;
      const int code = (int)(pk_bf16((float)lag, 0.f) & 0xffffu) - 0x4280;
      kv[i] = kt[lag < 128 ? lag : code];
    }
    kv[11] = kv[10];
#pragma unroll
    for (int i = 0; i < 6; ++i) ev[i] = sum2(kv[2 * i], kv[2 * i + 1]);
#pragma unroll
    for (int i = 0; i < 5; ++i) { pr[2 * i] = ev[i]; pr[2 * i + 1] = __builtin_amdgcn_alignbit(ev[i], ev[i + 1], 16); }
    emit(pp, pr);
  }
  {
    const int pp = 128 + (hw & 1) * 64 + lane;
    const unsigned ab = sum2(kt[384 + pp], kt[385 + pp]);
    const unsigned a0 = sum2(kt[pp == 128 ? 511 : 384 + pp], kt[384 + pp]);
    const unsigned aa = __builtin_amdgcn_perm(ab, ab, 0x03020302u), bb = __builtin_amdgcn_perm(ab, ab, 0x01000100u);
    const unsigned pr[10] = {a0, aa, aa, aa, aa, aa, (pp & 1) ? ab : aa, (pp & 1) ? bb : ab, bb, bb};
    emit(pp, pr);
  }
  if (lane < 32) {
    const int cpy = (hw & 1) * 2 + (lane >> 4), i = lane & 15;
    *(LAS v2u*)(kc + cpy * CV_KS + (i < 6 ? i : 512 + i) * 8) = (v2u){0u, 0u};
  }
}

DEV void g1_loader(const Params& p, Ts& ts, LAS char* sm, int rt, int ct, int rot, int hw, int lane) {
  const char* gb = hw ? (const char*)p.w1 + ct * 256 : (const char*)p.x + (size_t)rt * 128 * 1024;
  const int cstep = hw ? 64 * F * 2 : 128;
  unsigned v[4];
#pragma unroll
  for (int i = 0; i < 4; ++i) {
    const int rx = i * 8 + (lane >> 3), rw = i * 4 + (lane >> 4);
    v[i] = hw ? rw * (F * 2) + (((lane & 15) ^ ((rw & 3) << 2) ^ (i & 2)) << 4) : rx * 1024 + (((lane & 7) ^ ((rx >> 1) & 7)) << 4);
  }
  const int gstep = hw ? 16 * F * 2 : 32768;
  __builtin_amdgcn_s_setprio(LOADER_PRIO);
  __builtin_amdgcn_s_sleep(LOADER_DELAY);
  unsigned slot = (unsigned)(size_t)sm + hw * 16384;
#pragma unroll 1
  for (int c = 0; c < 8; ++c) {
    const char* g0 = gb + (size_t)(((c + rot) & 7) * cstep);
    const char *g1 = g0 + gstep, *g2 = g1 + gstep, *g3 = g2 + gstep;
#define GL4(S) "global_load_lds_dwordx4 %0, " S "\n\ts_add_u32 m0, m0, 0x400\n\ts_nop 0\n\tglobal_load_lds_dwordx4 %1, " S "\n\ts_add_u32 m0, m0, 0x400\n\ts_nop 0\n\t" \
               "global_load_lds_dwordx4 %2, " S "\n\ts_add_u32 m0, m0, 0x400\n\ts_nop 0\n\tglobal_load_lds_dwordx4 %3, " S "\n\ts_add_u32 m0, m0, 0x400\n\ts_nop 0\n\t"
    asm volatile("s_mov_b32 m0, %8\n\ts_nop 0\n\t" GL4("%4") GL4("%5") GL4("%6") GL4("%7")
                 :: "v"(v[0]), "v"(v[1]), "v"(v[2]), "v"(v[3]), "s"(g0), "s"(g1), "s"(g2), "s"(g3), "s"(slot) : "memory", "scc");
#undef GL4
    slot = slot + G1_SLOT >= (unsigned)(size_t)sm + G1_R * G1_SLOT ? slot - (G1_R - 1) * G1_SLOT : slot + G1_SLOT;
    if (c == 0) STAMP(p, 1);
    if (c == 1) STAMP(p, 2);
    if (c > 0) {
      asm volatile("s_waitcnt vmcnt(16)" ::: "memory");
      if (c == 1) STAMP(p, 3);
      if (c == 2) STAMP(p, 4);
      __builtin_amdgcn_s_barrier();
    }
  }
  drain();
  __builtin_amdgcn_s_barrier();
  __builtin_amdgcn_s_setprio(0);
}
struct G1Frag { v4u a[4], b[4]; };
DEV void g1_main(const Params& p, Ts& ts, LAS char* sm, rsrc_t rs, int rt, int ct, int w, int lane, v2u tag) {
  const int wf = w & 1, wt = w >> 1, g = lane >> 4, r16 = lane & 15;
  f32x4 acc[4][4] = {};
  const int q4 = r16 >> 2, p4 = lane & 3;
  int aoff[2], boff[2];
#pragma unroll
  for (int i = 0; i < 2; ++i) aoff[i] = 16384 + (8 * g + q4) * 256 + (((8 * wf + 4 * i + (p4 >> 1)) ^ (q4 << 2) ^ ((g & 1) << 1)) << 4) + 8 * (p4 & 1);
  const int tok0 = wt * 64 + r16;
#pragma unroll
  for (int s = 0; s < 2; ++s) boff[s] = tok0 * 128 + (((4 * s + g) ^ ((tok0 >> 1) & 7)) << 4);
  auto rd = [&](int c, int s) {
    G1Frag f;
    LAS char* base = sm + (c % G1_R) * G1_SLOT;
#pragma unroll
    for (int i = 0; i < 4; ++i) {
      const int o = (aoff[i >> 1] ^ ((i & 1) << 5)) + s * 8192;
      const v2u a0 = __builtin_bit_cast(v2u, __builtin_amdgcn_ds_read_tr16_b64_v4i16((LAS s16x4*)(base + o)));
      const v2u a1 = __builtin_bit_cast(v2u, __builtin_amdgcn_ds_read_tr16_b64_v4i16((LAS s16x4*)(base + o + 1024)));
      f.a[i] = (v4u){a0.x, a0.y, a1.x, a1.y};
      f.b[i] = *(const LAS v4u*)(base + boff[s] + 2048 * i);
    }
    return f;
  };
  G1Frag fr[16];
#pragma unroll
  for (int st = -1; st < 16; ++st) {
    const int ld = st + 1;
    if (ld < 16) {
      if ((ld & 1) == 0) lds_barrier();
      if (ld == 0) STAMP(p, 19);
      if (ld == 2) STAMP(p, 20);
      if (ld == 4) STAMP(p, 24);
      if (ld == 8) STAMP(p, 25);
      if (ld == 12) STAMP(p, 26);
      if (ld == 14) STAMP(p, 29);
      fr[ld] = rd(ld >> 1, ld & 1);
    }
    if (st >= 0) {
#pragma unroll
      for (int i = 0; i < 4; ++i)
#pragma unroll
        for (int j = 0; j < 4; ++j)
          acc[i][j] = __builtin_amdgcn_mfma_f32_16x16x32_bf16(__builtin_bit_cast(bf16x8, fr[st].a[i]), __builtin_bit_cast(bf16x8, fr[st].b[j]), acc[i][j], 0, 0, 0);
#pragma unroll
      for (int i = 0; i < 12; ++i) {
        __builtin_amdgcn_sched_group_barrier(0x8, 1, 0);
        __builtin_amdgcn_sched_group_barrier(0x100, 1, 0);
      }
      __builtin_amdgcn_sched_group_barrier(0x8, 4, 0);
    }
    __builtin_amdgcn_sched_barrier(0);
  }
  STAMP(p, 3);
  __builtin_amdgcn_s_setprio(2);
  lds_wait(sm, 8);
  unsigned gs[4], gc[4];
#pragma unroll
  for (int i = 0; i < 4; ++i) {
    gs[i] = *(const LAS unsigned*)(sm + GR + (tok0 + 16 * i) * 4);
    gc[i] = *(const LAS unsigned*)(sm + GR + 512 + (wf * 64 + r16 + 16 * i) * 4);
  }
  STAMP(p, 30);
  float c1[4], sd[4];
#pragma unroll
  for (int j = 0; j < 4; ++j) {
    const unsigned m2 = __builtin_amdgcn_perm(gs[j], gs[j], 0x01000100u) ^ 0x80008000u;
    const v2u bm = {g == 0 ? m2 : 0u, 0u};
#pragma unroll
    for (int i = 0; i < 4; ++i)
      acc[i][j] = __builtin_amdgcn_mfma_f32_16x16x16bf16_1k(__builtin_bit_cast(s16x4, (v2u){gc[i], 0u}), __builtin_bit_cast(s16x4, bm), acc[i][j], 0, 0, 0);
    sd[j] = bfhi(gs[j]);
    c1[j] = __builtin_amdgcn_rcpf(sd[j]) * -1.44269504088896f;
  }
  const int hbase = WS_HF + ((2 * rt + wt) * 56 + 4 * ct + 2 * wf) * 4096 + lane * 16;
#pragma unroll
  for (int m = 0; m < 2; ++m)
#pragma unroll
    for (int j = 0; j < 4; ++j) {
      v4u o;
#pragma unroll
      for (int e = 0; e < 4; ++e) {
        const f32x4 a4 = acc[2 * m + (e >> 1)][j];
        const f32x2 t = (e & 1) ? a4.zw : a4.xy;
        const f32x2 u = t * c1[j];
        const f32x2 ex = {__builtin_amdgcn_exp2f(u.x), __builtin_amdgcn_exp2f(u.y)};
        const f32x2 dn = __builtin_elementwise_fma(ex, (f32x2){sd[j], sd[j]}, (f32x2){sd[j], sd[j]});
        const f32x2 r = t * (f32x2){__builtin_amdgcn_rcpf(dn.x), __builtin_amdgcn_rcpf(dn.y)};
        o[e] = pk_bf16(r.x, r.y);
      }
      __builtin_amdgcn_raw_buffer_store_b128(o, rs, hbase + m * 4096 + j * 1024, 0, SC1);
      __builtin_amdgcn_sched_barrier(0);
    }
  STAMP(p, 4);
  publish(rs, sm, 1, 4, WS_FH + (rt * 16 + ct) * FSTR, tag, lane);
  __builtin_amdgcn_s_setprio(0);
}

DEV int ks_of(int w, int rl, int i) { return 7 * ((w + 2 * rl) & 7) + i; }
template <int LO, int HI> DEV void w2_load(rsrc_t rs, int woff, int w, int rl, v4u (&wr)[28]) {
#pragma unroll
  for (int i = LO; i < HI; ++i) wr[i] = __builtin_amdgcn_raw_buffer_load_b128(rs, woff + (i & 3) * 1024, ks_of(w, rl, i >> 2) * 4096, SC1);
}

DEV void conv(const Params& p, Ts& ts, LAS char* sm, rsrc_t rs, int b, int hw, int lane, v2u tag, int woff, int w, int rl, v4u (&wr)[28]) {
  const int ch = hw >> 1, eh = hw & 1, g = lane >> 4, r16 = lane & 15, d = chan_of(b, ch);
  LAS char* xs = sm + CV_X + ch * CV_XS;
  if (eh == 0 && lane < 34) *(LAS v4u*)(xs + (lane < 32 ? lane : 256 + lane) * 16) = (v4u){0u, 0u, 0u, 0u};
  lds_wait(sm, 3);
  STAMP(p, 14);
  v4u xv[2];
#pragma unroll
  for (int i = 0; i < 2; ++i)
    xv[i] = __builtin_amdgcn_raw_buffer_load_b128(rs, WS_XT + (hw * 64 + i * 32 + (lane >> 1)) * 8192 + (b & 7) * 1024 + (2 * (b >> 3) + (lane & 1)) * 16, 0, SC1);
  if (hw < 2) w2_load<0, W2_EARLY>(rs, woff, w, rl, wr);
  w2_load<W2_EARLY, 28>(rs, woff, w, rl, wr);
  __builtin_amdgcn_sched_barrier(0);
  STAMP(p, 15);
#pragma unroll
  for (int i = 0; i < 2; ++i) *(LAS v4u*)(sm + CV_X + (lane & 1) * CV_XS + 512 + (hw * 64 + i * 32 + (lane >> 1)) * 16) = xv[i];
  kexpand(sm, hw, lane);
  wave_sync(sm, 4, 4);
  STAMP(p, 6);
  const LAS char* ka = sm + KG_TAB + ch * 2 * KG_WAVE + (r16 & 3) * CV_KS + (517 - (r16 >> 2) + 2 * g) * 8;
  const LAS char* xa = xs + 512 + r16 * 32 + g * 16;
  auto lda = [&](const LAS char* ptr) {
    const v2u lo = *(const volatile LAS v2u*)ptr, hi = *(const volatile LAS v2u*)(ptr + 8);
    return __builtin_bit_cast(f16x8, (v4u){lo.x, lo.y, hi.x, hi.y});
  };
  f32x4 acc[8];
#pragma unroll
  for (int a = 0; a < 8; ++a) acc[a] = (f32x4){0.f, 0.f, 0.f, 0.f};
  const LAS char* ke = ka - 256 * eh;
  const LAS char* xe = xa - 256 * eh;
  static_for<0, 4>([&](auto ee) {
    constexpr int e = decltype(ee)::v;
    f16x8 A[8], B[8];
#pragma unroll
    for (int i = 0; i < 8; ++i) { A[i] = lda(ke - 64 * (e + 1) - 512 * i); B[i] = *(const LAS f16x8*)(xe - 64 * (e + 1) + 512 * i); }
#pragma unroll
    for (int a = 0; a < 8; ++a)
#pragma unroll
      for (int dd = 0; dd <= a; ++dd) acc[a] = __builtin_amdgcn_mfma_f32_16x16x32_f16(A[dd], B[a - dd], acc[a], 0, 0, 0);
    __builtin_amdgcn_sched_barrier(0);
  });
  LAS f32x4* ex = (LAS f32x4*)(sm + CV_E);
  f32x4 own[4];
#pragma unroll
  for (int i = 0; i < 4; ++i) {
    ex[(hw * 4 + i) * 64 + lane] = eh ? acc[i] : acc[4 + i];
    own[i] = eh ? acc[4 + i] : acc[i];
  }
  {
    const f16x8 A0 = lda(ka);
#pragma unroll
    for (int i = 0; i < 4; ++i) own[i] = __builtin_amdgcn_mfma_f32_16x16x32_f16(A0, *(const LAS f16x8*)(xa + 512 * (4 * eh + i)), own[i], 0, 0, 0);
  }
  STAMP(p, 7);
  wave_sync(sm, 6 + ch, 2);
  const f32x2 md = *(const LAS f32x2*)(sm + SY + 48 + ch * 8);
  const float mult = md.x, dsk = md.y;
#pragma unroll
  for (int i = 0; i < 4; ++i) {
    const f32x4 y = (own[i] + ex[((hw ^ 1) * 4 + i) * 64 + lane]) * mult;
    const int t0 = 256 * (4 * eh + i) + 16 * r16 + 4 * g;
    const v2u xr = *(const LAS v2u*)(xs + 512 + t0 * 2);
    const unsigned xr0 = xr.x, xr1 = xr.y;
    const f16x2 x0 = __builtin_bit_cast(f16x2, xr0), x1 = __builtin_bit_cast(f16x2, xr1);
    const unsigned y01 = pk_bf16(y[0], y[1]), y23 = pk_bf16(y[2], y[3]);
    const unsigned d01 = pk_bf16((float)x0.x * dsk, (float)x0.y * dsk), d23 = pk_bf16((float)x1.x * dsk, (float)x1.y * dsk);
    const v2u o = {pk_bf16(bflo(y01) + bflo(d01), bfhi(y01) + bfhi(d01)), pk_bf16(bflo(y23) + bflo(d23), bfhi(y23) + bfhi(d23))};
    __builtin_amdgcn_raw_buffer_store_b64(o, rs, WS_ST + d * 4096 + t0 * 2, 0, SC1);
  }
  publish(rs, sm, 2, 4, WS_FS + b * FSTR, tag, lane);
}

DEV void gemm2(const Params& p, Ts& ts, LAS char* sm, rsrc_t rs, int rt2, int ct2, int rl, bool isL, int w, int lane, v2u tag, v4u (&wr)[28]) {
  const int g = lane >> 4, hb = (lane >> 3) & 1, cb = lane & 7;
  if (w == 0) { wait_flags(rs, WS_FH + (rt2 >> 1) * 16 * FSTR, 14, tag, lane); lds_set(sm, 5); }
  else lds_wait(sm, 5);
  STAMP(p, 10);
  v4u ha[28];
  const int hoff = WS_HF + rt2 * 56 * 4096 + lane * 16;
  auto hload = [&](int i) { return __builtin_amdgcn_raw_buffer_load_b128(rs, hoff + (i & 3) * 1024, ks_of(w, rl, i >> 2) * 4096, SC1); };
#pragma unroll
  for (int i = 0; i < 16; ++i) ha[i] = hload(i);
  __builtin_amdgcn_sched_barrier(0);
  f32x4 acc[4][4] = {};
  auto mm = [&](int ks, int mt) {
#pragma unroll
    for (int t = 0; t < 4; ++t)
      acc[mt][t] = __builtin_amdgcn_mfma_f32_16x16x32_bf16(__builtin_bit_cast(bf16x8, ha[4 * ks + mt]), __builtin_bit_cast(bf16x8, wr[4 * ks + t]), acc[mt][t], 0, 0, 0);
  };
#pragma unroll
  for (int ks = 0; ks < 4; ++ks) {
#pragma unroll
    for (int mt = 0; mt < 4; ++mt) mm(ks, mt);
    __builtin_amdgcn_sched_barrier(0);
    if (ks < 2) {
#pragma unroll
      for (int i = 0; i < 6; ++i) { const int e = 6 * ks + i; ha[16 + 4 * (e % 3) + e / 3] = hload(16 + 4 * (e % 3) + e / 3); }
    }
    __builtin_amdgcn_sched_barrier(0);
  }
  lds_wait(sm, w < 4 ? 1 : 2, w < 4 && isL ? 0u : 4u);
  LAS f32x4* red = (LAS f32x4*)(sm + (w < 4 ? 32768 + w * 16384 : (w < 6 ? (w - 4) * 16384 : 98304 + (w - 6) * 16384)));
#pragma unroll
  for (int mt = 0; mt < 4; ++mt) {
#pragma unroll
    for (int ks = 4; ks < 7; ++ks) mm(ks, mt);
    if (mt > 0) {
#pragma unroll
      for (int t = 0; t < 4; ++t) red[((mt - 1) * 4 + t) * 64 + lane] = acc[mt - 1][t];
    }
#pragma unroll
    for (int i = 0; i < 4; ++i) { __builtin_amdgcn_sched_group_barrier(0x8, 3, 0); __builtin_amdgcn_sched_group_barrier(0x200, 1, 0); }
    __builtin_amdgcn_sched_barrier(0);
  }
#pragma unroll
  for (int t = 0; t < 4; ++t) red[(12 + t) * 64 + lane] = acc[3][t];
  STAMP(p, 11);
  const int tok = rt2 * 64 + 16 * w + 4 * g, chn = ct2 * 64 + 8 * cb + 4 * hb;
  v2u xv[4];
  f32x4 o[4];
  if (w < 4) {
#pragma unroll
    for (int i = 0; i < 4; ++i) xv[i] = *(const GAS v2u*)((const GAS char*)p.x + (size_t)(tok + i) * 1024 + chn * 2);
    wave_sync(sm, 10, 4);
#pragma unroll
    for (int t = 0; t < 4; ++t) {
      const LAS f32x4* r0 = (const LAS f32x4*)(sm + 32768) + (w * 4 + t) * 64 + lane;
      o[t] = (r0[0] + r0[1024]) + (r0[2048] + r0[3072]);
    }
    if (w == 0) {
      lds_wait(sm, 2, 4u);
      __builtin_amdgcn_s_sleep(16);
      wait_flags(rs, WS_FS + ct2 * 32 * FSTR, 32, tag, lane);
      lds_set(sm, 9);
    } else lds_wait(sm, 9);
#pragma unroll
    for (int i = 0; i < 2; ++i) {
      const int c = 16 * w + 8 * i + (lane >> 3);
      *(LAS v4u*)(sm + G2_S + c * 144 + (lane & 7) * 16) =
          __builtin_amdgcn_raw_buffer_load_b128(rs, WS_ST + (ct2 * 64 + c) * 4096 + rt2 * 128 + (lane & 7) * 16, 0, SC1);
    }
  }
  STAMP(p, 28);
  lds_barrier();
  STAMP(p, 12);
  if (w < 4) {
#pragma unroll
    for (int t = 0; t < 4; ++t) {
      const LAS f32x4* r0 = (const LAS f32x4*)sm + (w * 4 + t) * 64 + lane;
      o[t] += (r0[0] + r0[1024]) + (r0[6144] + r0[7168]);
    }
    v2u sv[4];
#pragma unroll
    for (int u = 0; u < 4; ++u) sv[u] = *(const LAS v2u*)(sm + G2_S + (8 * cb + 4 * hb + u) * 144 + 32 * w + 8 * g);
#pragma unroll
    for (int i = 0; i < 4; ++i) {
      v2u res;
#pragma unroll
      for (int u = 0; u < 2; ++u) {
        const unsigned ob = pk_bf16(o[2 * u][i], o[2 * u + 1][i]);
        const unsigned gb = pk_bf16(silu_f(bflo(ob)), silu_f(bfhi(ob)));
        const unsigned s0 = sv[2 * u][i >> 1], s1 = sv[2 * u + 1][i >> 1];
        const float sa = (i & 1) ? bfhi(s0) : bflo(s0), sb = (i & 1) ? bfhi(s1) : bflo(s1);
        const unsigned pr = pk_bf16(sa * bflo(gb), sb * bfhi(gb));
        const unsigned xx = xv[i][u];
        res[u] = pk_bf16(bflo(xx) + bflo(pr), bfhi(xx) + bfhi(pr));
      }
      *(GAS v2u*)((GAS char*)p.out + (size_t)(tok + i) * 1024 + chn * 2) = res;
    }
  }
  STAMP(p, 13);
}

__global__ void __launch_bounds__(512, 2) fused_kernel(Params p) {
  __shared__ __attribute__((aligned(16))) char smem[LDS_BYTES];
  LAS char* sm = (LAS char*)smem;
  const int tid = threadIdx.x, lane = tid & 63, w = __builtin_amdgcn_readfirstlane(tid >> 6);
  const int b = blockIdx.x, xcd = b & 7, idx = b >> 3;
  const bool isL = idx < 4;
  const int ii = idx - 4, rt = (xcd & 3) * 4 + (ii & 3), ct = (xcd >> 2) * 7 + (ii >> 2);
  const int rl = idx >> 3, rt2 = (xcd & 3) * 8 + (xcd >> 2) * 4 + rl, ct2 = idx & 7;
  const int t8 = (xcd & 3) * 64 + (xcd >> 2) * 32 + idx;
  Ts ts = {};
  STAMP(p, 0);
  const rsrc_t rs = __builtin_amdgcn_make_buffer_rsrc(p.ws, 0, WS_BYTES, 0x00020000);
  asm volatile("" ::"s"(p.x), "s"(p.lar), "s"(p.aim), "s"(p.cre), "s"(p.cim), "s"(p.ldt), "s"(p.dsk), "s"(p.w1), "s"(p.w2), "s"(p.ws));
  v2u ep;
  KPar kp;
  v4u early[8];
  unsigned pf;
  asm volatile("global_load_dwordx2 %0, %1, %2 sc1" : "=v"(ep) : "v"(WS_EPOCH), "s"(p.ws));
  if (w < 4) {
    const unsigned d2 = chan_of(b, w >> 1) * 2, o = d2 * NM + ((w & 1) * 64 + lane) * 2, o2 = o ^ 128;
    asm volatile("global_load_ushort %0, %8, %11\n\tglobal_load_ushort %1, %9, %12\n\tglobal_load_ushort %2, %9, %13\n\t"
                 "global_load_ushort %3, %9, %14\n\tglobal_load_ushort %4, %9, %15\n\tglobal_load_ushort %5, %10, %14\n\t"
                 "global_load_ushort %6, %10, %15\n\tglobal_load_ushort %7, %8, %16"
                 : "=&v"(kp.ldt), "=&v"(kp.lar), "=&v"(kp.aim), "=&v"(kp.c0), "=&v"(kp.c1), "=&v"(kp.c2), "=&v"(kp.c3), "=&v"(kp.dsk)
                 : "v"(d2), "v"(o), "v"(o2), "s"(p.ldt), "s"(p.lar), "s"(p.aim), "s"(p.cre), "s"(p.cim), "s"(p.dsk));
  }
  if (w >= 6) {
    const int piece = isL ? 0 : (b - 32) * 2;
    const char* sb = w == 6 ? (const char*)p.x + (size_t)t8 * 8192 : (const char*)p.w2 + (size_t)(piece % 56) * 32768 + (piece / 56) * 128;
    const unsigned o = w == 6 ? lane * 16 : (16 * ((lane >> 3) & 1) + 4 * (lane >> 4)) * 1024 + (lane & 7) * 16;
    const unsigned o2 = o + (w == 6 ? 4096 : 32768);
    asm volatile("global_load_dwordx4 %0, %8, %10\n\tglobal_load_dwordx4 %1, %8, %10 offset:1024\n\tglobal_load_dwordx4 %2, %8, %10 offset:2048\n\t"
                 "global_load_dwordx4 %3, %8, %10 offset:3072\n\tglobal_load_dwordx4 %4, %9, %10\n\tglobal_load_dwordx4 %5, %9, %10 offset:1024\n\t"
                 "global_load_dwordx4 %6, %9, %10 offset:2048\n\tglobal_load_dwordx4 %7, %9, %10 offset:3072"
                 : "=&v"(early[0]), "=&v"(early[1]), "=&v"(early[2]), "=&v"(early[3]), "=&v"(early[4]), "=&v"(early[5]), "=&v"(early[6]), "=&v"(early[7])
                 : "v"(o), "v"(o2), "s"(sb));
  }
  if ((w & 6) == 4) {
    const u64 pc = __builtin_amdgcn_s_getpc() & ~(u64)255;
    asm volatile("global_load_dword %0, %1, %2" : "=v"(pf) : "v"(min(idx * 1024 + (w - 4) * 512 + (lane & 3) * 128, CODE_BYTES - 128)), "s"(pc));
    if (isL) {
      drain();
#pragma unroll 1
      for (int i = 0; i < 8; ++i) bar();
    } else g1_loader(p, ts, sm, rt, ct, 2 * (ii & 3) + 2 * (ii >> 2), w - 4, lane);
    asm volatile("" ::"v"(pf));
  }
  __builtin_amdgcn_sched_barrier(0);
  const int woff = WS_W2 + ct2 * 56 * 4096 + lane * 16;
  v4u wr[28];
  auto make_tag = [&]() {
    unsigned lo, hi;
    asm volatile("v_readfirstlane_b32 %0, %2\n\tv_readfirstlane_b32 %1, %3" : "=s"(lo), "=s"(hi) : "v"(ep.x), "v"(ep.y));
    const u64 t = (((u64)hi << 32) | lo) + 1ull;
    return (v2u){(unsigned)t, (unsigned)(t >> 32)};
  };
  v2u tag;
  if (w == 4 || w == 5) {
    tag = make_tag();
  } else if (w < 4) {
    v4u cw[16];
    const int cg = (xcd & 3) * 4 + idx;
    if (isL) cs_load(p, (xcd >> 2) * 14 + min(cg, 13), w, lane, cw);
    if (w == 0 && lane < 12) *(LAS unsigned*)(sm + SY + lane * 4) = 0u;
    if (w == 1 && lane < 8) *(LAS unsigned*)(sm + KG_K + (lane >> 2) * KG_KCH + (lane & 3) * 4) = 0u;
    __builtin_amdgcn_sched_barrier(0);
    drain();
    __builtin_amdgcn_sched_barrier(0);
    asm volatile("" : "+v"(kp.ldt), "+v"(kp.lar), "+v"(kp.aim), "+v"(kp.c0), "+v"(kp.c1), "+v"(kp.c2), "+v"(kp.c3), "+v"(kp.dsk));
    tag = make_tag();
    STAMP(p, 1);
    if (isL) cs_role(sm, rs, (xcd >> 2) * 14 + cg, cg < 14, w, lane, tag, cw);
    STAMP(p, 16);
    kgen(p, ts, sm, kp, w, lane);
    STAMP(p, 2);
    if (isL) {
      asm volatile("s_waitcnt lgkmcnt(0)" ::: "memory");
#pragma unroll 1
      for (int i = 0; i < 7; ++i) bar();
    } else g1_main(p, ts, sm, rs, rt, ct, w, lane, tag);
    STAMP(p, 5);
    w2_load<0, 28>(rs, woff, w, rl, wr);
    __builtin_amdgcn_sched_barrier(0);
    STAMP(p, 23);
  } else {
    drain();
    __builtin_amdgcn_sched_barrier(0);
    asm volatile("" : "+v"(early[0]), "+v"(early[1]), "+v"(early[2]), "+v"(early[3]), "+v"(early[4]), "+v"(early[5]), "+v"(early[6]), "+v"(early[7]));
    tag = make_tag();
    STAMP(p, 1);
    if (w == 6) ln_wave(p, ts, rs, t8, lane, tag, early);
    else if (!isL) { w2_piece_store(rs, (b - 32) * 2, lane, early); w2_piece_store(rs, (b - 32) * 2 + 1, lane, early + 4); }
    STAMP(p, 27);
    bar();
    if (!isL) bar();
    drain();
    if (lane == 0 && (w == 6 || !isL)) __builtin_amdgcn_raw_buffer_store_b64(tag, rs, w == 6 ? WS_FX + b * FSTR : WS_FW + (b - 32) * FSTR, 0, SC1);
    STAMP(p, 22);
    if (isL) bar();
    bar(); bar(); bar();
    if (w == 7) {
      const int l28 = lane < 28 ? lane : (isL ? min(lane, 55) + 84 : 27);
      bool first = true;
#pragma unroll 1
      for (unsigned spins = 0; spins < SPIN_CAP; ++spins) {
        asm volatile("" ::: "memory");
        const v2u f = __builtin_amdgcn_raw_buffer_load_b64(rs, WS_FW + (ct2 * 28 + l28) * FSTR, 0, SC1);
        __builtin_amdgcn_sched_barrier(0);
        if (first) { bar(); first = false; }
        if (__all(f.x == tag.x && f.y == tag.y)) break;
        __builtin_amdgcn_s_sleep(8);
      }
    } else bar();
    bar();
    const int poff = w == 7 ? WS_FX + lane * FSTR : WS_GS + (isL ? 0 : rt * 1024) + lane * 8;
    const int pst1 = w == 7 ? 64 * FSTR : 512, pst2 = w == 7 ? 128 * FSTR : WS_GC - WS_GS + (isL ? 0 : (ct - rt) * 1024);
    const unsigned tx = w == 7 ? tag.y : tag.x, mx = w == 7 ? ~0u : 0u;
    v2u f[4];
#pragma unroll
    for (int i = 0; i < 4; ++i) f[i] = __builtin_amdgcn_raw_buffer_load_b64(rs, poff, (i & 1) * pst1 + (i >> 1) * pst2, SC1);
    w2_load<0, W2_EARLY>(rs, woff, w, rl, wr);
    __builtin_amdgcn_sched_barrier(0);
    bar();
#pragma unroll 1
    for (unsigned spins = 0; spins < SPIN_CAP; ++spins) {
      unsigned bad = 0;
#pragma unroll
      for (int i = 0; i < 4; ++i) bad |= (f[i].y ^ tx) | ((f[i].x ^ tag.x) & mx);
      if (__all(bad == 0u)) break;
      __builtin_amdgcn_s_sleep(8);
      asm volatile("" ::: "memory");
#pragma unroll
      for (int i = 0; i < 4; ++i) f[i] = __builtin_amdgcn_raw_buffer_load_b64(rs, poff, (i & 1) * pst1 + (i >> 1) * pst2, SC1);
    }
    if (w == 6) {
#pragma unroll
      for (int i = 0; i < 4; ++i) *(LAS unsigned*)(sm + GR + i * 256 + lane * 4) = f[i].x;
      asm volatile("" ::: "memory");
    }
    lds_set(sm, w == 6 ? 8 : 3);
    if (b == 0 && w == 7 && lane == 0) __builtin_amdgcn_raw_buffer_store_b64(tag, rs, WS_EPOCH, 0, SC1);
  }
  if (w >= 4) {
    STAMP(p, 9);
    conv(p, ts, sm, rs, b, w - 4, lane, tag, woff, w, rl, wr);
    STAMP(p, 8);
  }
  gemm2(p, ts, sm, rs, rt2, ct2, rl, isL, w, lane, tag, wr);
}

extern "C" void kernel_launch(void* const* d_in, const int* in_sizes, int n_in, void* d_out, int out_size, void* d_ws, size_t ws_size,
                              hipStream_t stream) {
  Params p;
  p.x = (const u16*)d_in[0]; p.lar = (const u16*)d_in[1]; p.aim = (const u16*)d_in[2]; p.cre = (const u16*)d_in[3];
  p.cim = (const u16*)d_in[4]; p.ldt = (const u16*)d_in[5]; p.dsk = (const u16*)d_in[6]; p.w1 = (const u16*)d_in[8];
  p.w2 = (const u16*)d_in[9]; p.out = (u16*)d_out; p.ws = (char*)d_ws;
  fused_kernel<<<256, 512, 0, stream>>>(p);
}
```

```cpp
#define STAMPS
#define PROBE 0
#define PROBE_B 100
#define PROBE_W 0
#include <hip/hip_runtime.h>
#include <hip/hip_bf16.h>
#include <stdint.h>

typedef unsigned short u16;
typedef unsigned long long u64;
typedef unsigned v4u __attribute__((ext_vector_type(4)));
typedef unsigned v2u __attribute__((ext_vector_type(2)));
typedef float f32x2 __attribute__((ext_vector_type(2)));
typedef float f32x4 __attribute__((ext_vector_type(4)));
typedef float f32x16 __attribute__((ext_vector_type(16)));
typedef short s16x4 __attribute__((ext_vector_type(4)));
typedef __bf16 bf16x2 __attribute__((ext_vector_type(2)));
typedef __bf16 bf16x8 __attribute__((ext_vector_type(8)));
typedef _Float16 f16x2 __attribute__((ext_vector_type(2)));
typedef _Float16 f16x8 __attribute__((ext_vector_type(8)));
typedef __amdgpu_buffer_rsrc_t rsrc_t;
#define GAS __attribute__((address_space(1)))
#define LAS __attribute__((address_space(3)))
#define DEV __device__ __forceinline__

constexpr int L = 2048, D = 512, NM = 128, F = 1792;
constexpr int WS_EPOCH = 0;
constexpr int FSTR = 64;
constexpr int WS_FH = 4096;
constexpr int WS_FX = WS_FH + 256 * FSTR;
constexpr int WS_FW = WS_FX + 256 * FSTR;
constexpr int WS_FS = WS_FW + 256 * FSTR;
constexpr int WS_GS = WS_FS + 256 * FSTR;
constexpr int WS_GC = WS_GS + L * 8;
constexpr int WS_XT = 1 << 17;
constexpr int WS_ST = WS_XT + (2 << 20);
constexpr int WS_W2 = WS_ST + (2 << 20);
constexpr int WS_HF = WS_W2 + (2 << 20);
constexpr int WS_BYTES = WS_HF + L * F * 2;
#define LOADER_PRIO 0
#define LOADER_DELAY 10
constexpr int SC1 = 16;
constexpr int W2_EARLY = 10;
constexpr int CODE_BYTES = 27 * 1024;
constexpr unsigned SPIN_CAP = 1u << 16;
constexpr int G1_SLOT = 32768, G1_R = 3;
constexpr int KG_ROW = 272;
constexpr int KG_WAVE = 53 * KG_ROW;
constexpr int KG_TAB = G1_R * G1_SLOT;
constexpr int CV_KS = 4288;
constexpr int KG_K = KG_TAB + 4 * KG_WAVE;
constexpr int KG_KCH = 676 * 4;
constexpr int SY = KG_K + 2 * KG_KCH;
constexpr int GR = SY + 64;
constexpr int LDS_USED = GR + 1024;
constexpr int G2_S = 144384;
constexpr int CV_X = 0;
constexpr int CV_XS = (256 + 2048 + 16) * 2;
constexpr int CV_E = 2 * CV_XS;
constexpr int LDS_BYTES = 160 * 1024;
static_assert(WS_GC + F * 8 <= WS_XT && G2_S >= KG_TAB + 2 * KG_WAVE + 4 * CV_KS && G2_S + 64 * 144 <= KG_K, "maps");
static_assert(4 * CV_KS <= 2 * KG_WAVE && (2 * KG_WAVE) % 16 == 0 && LDS_USED <= LDS_BYTES && CV_E % 16 == 0 && CV_E + 16384 <= G1_SLOT, "lds map");

DEV float bf2f(unsigned v) { return __uint_as_float(v << 16); }
DEV float bflo(unsigned v) { return __uint_as_float(v << 16); }
DEV float bfhi(unsigned v) { return __uint_as_float(v & 0xffff0000u); }
DEV unsigned pk_bf16(float a, float b) { f32x2 f = {a, b}; return __builtin_bit_cast(unsigned, __builtin_convertvector(f, bf16x2)); }
DEV unsigned pk_f16(float a, float b) { f32x2 f = {a, b}; return __builtin_bit_cast(unsigned, __builtin_convertvector(f, f16x2)); }
DEV float rbf(float a) { return bflo(pk_bf16(a, a) & 0xffffu); }
DEV float dot2bf(unsigned a, unsigned b, float c) {
  return __builtin_amdgcn_fdot2_f32_bf16(__builtin_bit_cast(bf16x2, a), __builtin_bit_cast(bf16x2, b), c, false);
}
DEV float silu_f(float v) { return v * __builtin_amdgcn_rcpf(1.f + __builtin_amdgcn_exp2f(v * -1.44269504088896f)); }
DEV void lds_barrier() { asm volatile("s_waitcnt lgkmcnt(0)" ::: "memory"); __builtin_amdgcn_s_barrier(); asm volatile("" ::: "memory"); }
DEV void drain() { asm volatile("s_waitcnt vmcnt(0)" ::: "memory"); }
template <int CTRL> DEV float dpp_add(float v) {
  return v + __builtin_bit_cast(float, __builtin_amdgcn_update_dpp(0, __builtin_bit_cast(int, v), CTRL, 0xf, 0xf, true));
}
DEV float row16_sum(float v) { v = dpp_add<0xb1>(v); v = dpp_add<0x4e>(v); v = dpp_add<0x124>(v); return dpp_add<0x128>(v); }
DEV float swap32_add(float a, float b) {
  auto r = __builtin_amdgcn_permlane32_swap(__float_as_uint(a), __float_as_uint(b), false, false);
  return __uint_as_float(r[0]) + __uint_as_float(r[1]);
}
DEV float swap16_add(float a, float b) {
  auto r = __builtin_amdgcn_permlane16_swap(__float_as_uint(a), __float_as_uint(b), false, false);
  return __uint_as_float(r[0]) + __uint_as_float(r[1]);
}
DEV float rdlane(float v, int l) { return __uint_as_float(__builtin_amdgcn_readlane(__float_as_uint(v), l)); }
template <int I> struct IC { static constexpr int v = I; };
template <int B, int E, class Fn> DEV void static_for(Fn&& f) { if constexpr (B < E) { f(IC<B>{}); static_for<B + 1, E>(f); } }

struct Ts { unsigned t[32]; };
#define STAMP(p, i) do { __builtin_amdgcn_sched_barrier(0); ts.t[i] = (unsigned)__builtin_amdgcn_s_memrealtime(); __builtin_amdgcn_sched_barrier(0); } while (0)

struct Params {
  const u16 *x, *lar, *aim, *cre, *cim, *ldt, *dsk, *w1, *w2;
  u16* out;
  char* ws;
};

DEV void wait_flags(rsrc_t rs, int off, int n, v2u tag, int lane) {
#pragma unroll 1
  for (unsigned spins = 0; spins < SPIN_CAP; ++spins) {
    asm volatile("" ::: "memory");
    const v2u v = __builtin_amdgcn_raw_buffer_load_b64(rs, off + min(lane, n - 1) * FSTR, 0, SC1);
    if (__all(v.x == tag.x && v.y == tag.y)) break;
    __builtin_amdgcn_s_sleep(4);
  }
  __builtin_amdgcn_fence(__ATOMIC_ACQUIRE, "wavefront");
}
DEV void publish(rsrc_t rs, LAS char* sm, int word, unsigned nwaves, int off, v2u tag, int lane) {
  drain();
  if (lane == 0) {
    const unsigned old = __hip_atomic_fetch_add((LAS unsigned*)(sm + SY) + word, 1u, __ATOMIC_RELAXED, __HIP_MEMORY_SCOPE_WORKGROUP);
    if (old == nwaves - 1) __builtin_amdgcn_raw_buffer_store_b64(tag, rs, off, 0, SC1);
  }
}
DEV void wave_sync(LAS char* sm, int word, unsigned n) {
  asm volatile("s_waitcnt lgkmcnt(0)" ::: "memory");
  volatile LAS unsigned* c = (volatile LAS unsigned*)(sm + SY) + word;
  if ((threadIdx.x & 63) == 0) __hip_atomic_fetch_add((LAS unsigned*)c, 1u, __ATOMIC_RELAXED, __HIP_MEMORY_SCOPE_WORKGROUP);
#pragma unroll 1
  for (unsigned spins = 0; spins < 16 * SPIN_CAP && __builtin_amdgcn_readfirstlane(*c) != n; ++spins) {}
  asm volatile("" ::: "memory");
}

DEV void lds_set(LAS char* sm, int word) { *((volatile LAS unsigned*)(sm + SY) + word) = 1u; }
DEV void lds_wait(LAS char* sm, int word, unsigned want = 1u) {
  volatile LAS unsigned* c = (volatile LAS unsigned*)(sm + SY) + word;
#pragma unroll 1
  for (unsigned spins = 0; spins < 16 * SPIN_CAP && __builtin_amdgcn_readfirstlane(*c) != want; ++spins) __builtin_amdgcn_s_sleep(1);
  __builtin_amdgcn_fence(__ATOMIC_ACQUIRE, "wavefront");
  asm volatile("" ::: "memory");
}
DEV void bar() { asm volatile("" ::: "memory"); __builtin_amdgcn_s_barrier(); asm volatile("" ::: "memory"); }

DEV void ln_wave(const Params& p, Ts& ts, rsrc_t rs, int t8, int lane, v2u tag, const v4u (&xr)[8]) {
  float s[8], q[8], t[4], rz[2], mz[2];
#pragma unroll
  for (int r = 0; r < 8; ++r) {
    s[r] = 0.f; q[r] = 0.f;
#pragma unroll
    for (int j = 0; j < 4; ++j) { s[r] = dot2bf(xr[r][j], 0x3f803f80u, s[r]); q[r] = dot2bf(xr[r][j], xr[r][j], q[r]); }
  }
#pragma unroll
  for (int j = 0; j < 4; ++j) { t[j] = swap32_add(s[j], s[j + 4]); q[j] = swap32_add(q[j], q[j + 4]); }
#pragma unroll
  for (int u = 0; u < 2; ++u) {
    const float mean = row16_sum(swap16_add(t[u], t[u + 2])) * (1.f / D);
    const float var = rbf(fmaxf(row16_sum(swap16_add(q[u], q[u + 2])) * (1.f / D) - mean * mean, 0.f));
    const unsigned ms = pk_bf16(mean, __builtin_amdgcn_sqrtf(rbf(var + 1e-5f)));
    if ((lane & 15) == 0) __builtin_amdgcn_raw_buffer_store_b64((v2u){ms, tag.x}, rs, WS_GS + (t8 * 8 + u + 2 * (lane >> 4)) * 8, 0, SC1);
    rz[u] = __builtin_amdgcn_rcpf(bfhi(ms));
    mz[u] = -bflo(ms) * rz[u];
  }
  float rr[8], mm[8];
#pragma unroll
  for (int r = 0; r < 8; ++r) { rr[r] = rdlane(rz[r & 1], 16 * (r >> 1)); mm[r] = rdlane(mz[r & 1], 16 * (r >> 1)); }
  STAMP(p, 21);
#pragma unroll
  for (int j = 0; j < 4; ++j) {
    __builtin_amdgcn_sched_barrier(0);
    f32x2 v[8];
#pragma unroll
    for (int r = 0; r < 8; ++r) v[r] = __builtin_elementwise_fma((f32x2){bflo(xr[r][j]), bfhi(xr[r][j])}, (f32x2){rr[r], rr[r]}, (f32x2){mm[r], mm[r]});
#pragma unroll
    for (int e = 0; e < 2; ++e) {
      v4u o;
#pragma unroll
      for (int i = 0; i < 4; ++i) o[i] = pk_f16(v[2 * i][e], v[2 * i + 1][e]);
      __builtin_amdgcn_raw_buffer_store_b128(o, rs, WS_XT + t8 * 8192 + (2 * j + e) * 1024 + lane * 16, 0, SC1);
    }
  }
}

DEV void cs_load(const Params& p, int cb, int w, int lane, v4u (&wr)[16]) {
#pragma unroll
  for (int i = 0; i < 16; ++i) wr[i] = *(const GAS v4u*)((const GAS char*)p.w1 + (size_t)(w * 128 + i * 8 + (lane >> 3)) * (F * 2) + cb * 128 + (lane & 7) * 16);
}
DEV void cs_role(LAS char* sm, rsrc_t rs, int tb, bool act, int w, int lane, v2u tag, const v4u (&wr)[16]) {
  unsigned sel_lo = 0x00003f80u, sel_hi = 0x3f800000u;
  asm volatile("" : "+s"(sel_lo), "+s"(sel_hi));
  float c[8] = {0.f, 0.f, 0.f, 0.f, 0.f, 0.f, 0.f, 0.f}, t[4], y[2];
#pragma unroll
  for (int i = 0; i < 16; ++i)
#pragma unroll
    for (int j = 0; j < 4; ++j) { c[2 * j] = dot2bf(sel_lo, wr[i][j], c[2 * j]); c[2 * j + 1] = dot2bf(sel_hi, wr[i][j], c[2 * j + 1]); }
#pragma unroll
  for (int j = 0; j < 4; ++j) t[j] = swap32_add(c[j], c[j + 4]);
#pragma unroll
  for (int u = 0; u < 2; ++u) y[u] = dpp_add<0x128>(swap16_add(t[u], t[u + 2]));
  if ((lane & 8) == 0) *(LAS f32x2*)(sm + 65536 + (w * 64 + (lane & 7) * 8 + 2 * (lane >> 4)) * 4) = (f32x2){y[0], y[1]};
  lds_barrier();
  if (w == 0 && act) {
    float cs = 0.f;
#pragma unroll
    for (int ww = 0; ww < 4; ++ww) cs += *(const LAS float*)(sm + 65536 + (ww * 64 + lane) * 4);
    const float hi = rbf(cs);
    __builtin_amdgcn_raw_buffer_store_b64((v2u){pk_bf16(hi, cs - hi), tag.x}, rs, WS_GC + (tb * 64 + lane) * 8, 0, SC1);
  }
}

DEV void w2_piece_store(rsrc_t rs, int piece, int lane, const v4u* wr) {
  unsigned P[4][2], Q[4][2];
#pragma unroll
  for (int i = 0; i < 4; ++i)
#pragma unroll
    for (int u = 0; u < 2; ++u) {
      const unsigned X = wr[i][u], Y = wr[i][2 + u];
      P[i][u] = __builtin_amdgcn_update_dpp(X, Y, 0x128, 0xf, 0xc, false);
      Q[i][u] = __builtin_amdgcn_update_dpp(Y, X, 0x128, 0xf, 0x3, false);
    }
#pragma unroll
  for (int t = 0; t < 4; ++t) {
    const unsigned sel = (t & 1) ? 0x07060302u : 0x05040100u;
    const int u = t >> 1;
    const v4u B = {__builtin_amdgcn_perm(P[1][u], P[0][u], sel), __builtin_amdgcn_perm(P[3][u], P[2][u], sel),
                   __builtin_amdgcn_perm(Q[1][u], Q[0][u], sel), __builtin_amdgcn_perm(Q[3][u], Q[2][u], sel)};
    __builtin_amdgcn_raw_buffer_store_b128(B, rs, WS_W2 + piece * 4096 + t * 1024 + lane * 16, 0, SC1);
  }
}

struct KPar { unsigned ldt, lar, aim, c0, c1, c2, c3, dsk; };
DEV int chan_of(int b, int ch) { return 16 * (b >> 3) + 8 * ch + (b & 7); }
DEV f32x2 crot(f32x2 w) { return (f32x2){-w.y, w.x}; }
DEV f32x2 cmul(f32x2 a, f32x2 w, f32x2 wr) { return __builtin_elementwise_fma(a.yy, wr, a.xx * w); }
DEV f32x2 cexp_at(float a2, float rv, float pos) {
  const float e = __builtin_amdgcn_exp2f(a2 * pos);
  const float ph = __builtin_amdgcn_fractf(rv * pos);
  return (f32x2){__builtin_amdgcn_cosf(ph), __builtin_amdgcn_sinf(ph)} * e;
}
DEV float exp_acc(float x) {
  const float t = x * 1.44269504088896f;
  const float r = fmaf(x, 1.92596299112661e-8f, fmaf(x, 1.44269504088896f, -t));
  const float e = __builtin_amdgcn_exp2f(t);
  return fmaf(e, r * 0.693147180559945f, e);
}
DEV void kgen(const Params& p, Ts& ts, LAS char* sm, const KPar& k, int w, int lane) {
  const int ch = w >> 1;
  const float dt = rbf(exp_acc(bf2f(k.ldt)));
  const float a2 = -rbf(exp_acc(bf2f(k.lar))) * dt * 1.44269504088896f;
  const float rv = bf2f(k.aim) * dt * 0.159154943091895f;
  LAS char* t = sm + KG_TAB + w * KG_WAVE + lane * 4;
  auto put = [&](int row, f32x2 u) { *(LAS unsigned*)(t + row * KG_ROW) = pk_f16(u.x, u.y); };
  {
    const f32x2 w8 = cexp_at(a2, -rv, 8.f), w8r = crot(w8);
    const f32x2 w64 = cexp_at(a2, -rv, 64.f), w64r = crot(w64), w128 = cmul(w64, w64, w64r);
    f32x2 v[4] = {{1.f, 0.f}, w64, w128, cmul(w128, w64, w64r)};
#pragma unroll
    for (int i = 0; i < 8; ++i)
#pragma unroll
      for (int c = 0; c < 4; ++c) { put(21 + 8 * c + i, v[c]); if (i < 7) v[c] = cmul(v[c], w8, w8r); }
  }
  {
    unsigned mx = max(max(k.c0 & 0x7fffu, k.c1 & 0x7fffu), max(k.c2 & 0x7fffu, k.c3 & 0x7fffu));
    mx = max(mx, (unsigned)__builtin_amdgcn_update_dpp(0, (int)mx, 0xb1, 0xf, 0xf, true));
    mx = max(mx, (unsigned)__builtin_amdgcn_update_dpp(0, (int)mx, 0x4e, 0xf, 0xf, true));
    mx = max(mx, (unsigned)__builtin_amdgcn_update_dpp(0, (int)mx, 0x124, 0xf, 0xf, true));
    mx = max(mx, (unsigned)__builtin_amdgcn_update_dpp(0, (int)mx, 0x128, 0xf, 0xf, true));
    unsigned ex = max(max(__builtin_amdgcn_readlane(mx, 0), __builtin_amdgcn_readlane(mx, 16)), max(__builtin_amdgcn_readlane(mx, 32), __builtin_amdgcn_readlane(mx, 48))) >> 7;
    ex = min(max(ex, 1u), 253u);
    const float sc = __uint_as_float((254u - ex) << 23), isc = __uint_as_float(ex << 23);
    if (lane == 0) *(LAS f32x2*)(sm + SY + 48 + ch * 8) = (f32x2){2.f * dt * isc, bf2f(k.dsk)};
    const f32x2 c = (f32x2){bf2f(k.c0), bf2f(k.c1)} * sc;
    const f32x2 w1 = cexp_at(a2, rv, 1.f), e256 = cexp_at(a2, rv, 256.f), e256r = crot(e256), e512 = cmul(e256, e256, e256r), e1024 = cmul(e512, e512, crot(e512));
    const f32x2 w1r = crot(w1), w2 = cmul(w1, w1, w1r), w2r = crot(w2), w4 = cmul(w2, w2, w2r), w4r = crot(w4), cr = crot(c);
    f32x2 u = c, u4 = cmul(c, w4, w4r), u2 = cmul(e256, c, cr);
#pragma unroll
    for (int i = 0; i < 4; ++i) {
      put(i, u); put(4 + i, u4); put(8 + i, u2);
      if (i < 3) { u = cmul(u, w1, w1r); u4 = cmul(u4, w1, w1r); u2 = cmul(u2, w2, w2r); }
    }
    const f32x2 u512 = cmul(e512, c, cr), u768 = cmul(u512, e256, e256r), u1024 = cmul(e1024, c, cr), u1024r = crot(u1024);
    const f32x2 u1536 = cmul(e512, u1024, u1024r);
    put(12, u512); put(13, cmul(u512, w4, w4r)); put(14, u768); put(15, cmul(u768, w4, w4r));
    put(16, u1024); put(17, cmul(u1024, e256, e256r)); put(18, u1536); put(19, cmul(u1536, e256, e256r)); put(20, cmul(e1024, u1024, u1024r));
  }
  asm volatile("" ::: "memory");
  STAMP(p, 17);
  {
    const int h = lane >> 5, b = lane & 31;
    const LAS char* ua = sm + KG_TAB + w * KG_WAVE + b * KG_ROW + h * 16;
    f32x16 acc = {};
    f16x8 fa[8], fb[8];
#pragma unroll
    for (int s = 0; s < 8; ++s) { fa[s] = *(const LAS f16x8*)(ua + s * 32); fb[s] = *(const LAS f16x8*)(ua + 21 * KG_ROW + s * 32); }
    __builtin_amdgcn_sched_barrier(0);
#pragma unroll
    for (int s = 0; s < 8; ++s) acc = __builtin_amdgcn_mfma_f32_32x32x16_f16(fa[s], fb[s], acc, 0, 0, 0);
    STAMP(p, 18);
    LAS _Float16* kt = (LAS _Float16*)(sm + KG_K + ch * KG_KCH) + 8 + (w & 1);
    auto add = [&](int i, float v) { kt[2 * i] = (_Float16)v; };
    const int i0 = 4 * h + 8 * b, i1 = h ? 384 + 2 * b : 256 + 4 * b, i1s = h ? 64 : 2;
#pragma unroll
    for (int r = 0; r < 4; ++r) add(i0 + r, acc[r]);
    add(i1, acc[4]); add(i1 + 1, acc[5]); add(i1 + i1s, acc[6]); add(i1 + i1s + 1, acc[7]);
    add(512 + 128 * h + b, acc[8]);
    if (h == 0) { add(544 + b, acc[9]); add(576 + b, acc[10]); add(608 + b, acc[11]); }
  }
}
DEV void kexpand(LAS char* sm, int hw, int lane) {
  const int ch = hw >> 1;
  const LAS unsigned* kt = (const LAS unsigned*)(sm + KG_K + ch * KG_KCH) + 4;
  LAS char* kc = sm + KG_TAB + ch * 2 * KG_WAVE;
  auto sum2 = [&](unsigned hi, unsigned lo) {
    const unsigned a = __builtin_amdgcn_perm(hi, lo, 0x05040100u), b = __builtin_amdgcn_perm(hi, lo, 0x07060302u);
    return __builtin_bit_cast(unsigned, __builtin_bit_cast(f16x2, a) + __builtin_bit_cast(f16x2, b));
  };
  auto emit = [&](int pp, const unsigned (&pr)[10]) {
#pragma unroll
    for (int sg = 0; sg < 4; ++sg) *(LAS v4u*)(kc + sg * CV_KS + (516 - 2 * pp) * 8) = (v4u){pr[6 + sg], pr[4 + sg], pr[2 + sg], pr[sg]};
  };
  {
    const int pp = (hw & 1) * 64 + lane;
    unsigned kv[12], pr[10], ev[6];
#pragma unroll
    for (int i = 0; i < 11; ++i) {
      const int lag = 8 * pp - 3 + i;
      const int code = (int)(pk_bf16((float)lag, 0.f) & 0xffffu) - 0x4280;
      kv[i] = kt[lag < 128 ? lag : code];
    }
    kv[11] = kv[10];
#pragma unroll
    for (int i = 0; i < 6; ++i) ev[i] = sum2(kv[2 * i], kv[2 * i + 1]);
#pragma unroll
    for (int i = 0; i < 5; ++i) { pr[2 * i] = ev[i]; pr[2 * i + 1] = __builtin_amdgcn_alignbit(ev[i], ev[i + 1], 16); }
    emit(pp, pr);
  }
  {
    const int pp = 128 + (hw & 1) * 64 + lane;
    const unsigned ab = sum2(kt[384 + pp], kt[385 + pp]);
    const unsigned a0 = sum2(kt[pp == 128 ? 511 : 384 + pp], kt[384 + pp]);
    const unsigned aa = __builtin_amdgcn_perm(ab, ab, 0x03020302u), bb = __builtin_amdgcn_perm(ab, ab, 0x01000100u);
    const unsigned pr[10] = {a0, aa, aa, aa, aa, aa, (pp & 1) ? ab : aa, (pp & 1) ? bb : ab, bb, bb};
    emit(pp, pr);
  }
  if (lane < 32) {
    const int cpy = (hw & 1) * 2 + (lane >> 4), i = lane & 15;
    *(LAS v2u*)(kc + cpy * CV_KS + (i < 6 ? i : 512 + i) * 8) = (v2u){0u, 0u};
  }
}

DEV void g1_loader(const Params& p, Ts& ts, LAS char* sm, int rt, int ct, int rot, int hw, int lane) {
  const char* gb = hw ? (const char*)p.w1 + ct * 256 : (const char*)p.x + (size_t)rt * 128 * 1024;
  const int cstep = hw ? 64 * F * 2 : 128;
  unsigned v[4];
#pragma unroll
  for (int i = 0; i < 4; ++i) {
    const int rx = i * 8 + (lane >> 3), rw = i * 4 + (lane >> 4);
    v[i] = hw ? rw * (F * 2) + (((lane & 15) ^ ((rw & 3) << 2) ^ (i & 2)) << 4) : rx * 1024 + (((lane & 7) ^ ((rx >> 1) & 7)) << 4);
  }
  const int gstep = hw ? 16 * F * 2 : 32768;
  __builtin_amdgcn_s_setprio(LOADER_PRIO);
  __builtin_amdgcn_s_sleep(LOADER_DELAY);
  unsigned slot = (unsigned)(size_t)sm + hw * 16384;
#pragma unroll 1
  for (int c = 0; c < 8; ++c) {
    const char* g0 = gb + (size_t)(((c + rot) & 7) * cstep);
    const char *g1 = g0 + gstep, *g2 = g1 + gstep, *g3 = g2 + gstep;
#define GL4(S) "global_load_lds_dwordx4 %0, " S "\n\ts_add_u32 m0, m0, 0x400\n\ts_nop 0\n\tglobal_load_lds_dwordx4 %1, " S "\n\ts_add_u32 m0, m0, 0x400\n\ts_nop 0\n\t" \
               "global_load_lds_dwordx4 %2, " S "\n\ts_add_u32 m0, m0, 0x400\n\ts_nop 0\n\tglobal_load_lds_dwordx4 %3, " S "\n\ts_add_u32 m0, m0, 0x400\n\ts_nop 0\n\t"
    asm volatile("s_mov_b32 m0, %8\n\ts_nop 0\n\t" GL4("%4") GL4("%5") GL4("%6") GL4("%7")
                 :: "v"(v[0]), "v"(v[1]), "v"(v[2]), "v"(v[3]), "s"(g0), "s"(g1), "s"(g2), "s"(g3), "s"(slot) : "memory", "scc");
#undef GL4
    slot = slot + G1_SLOT >= (unsigned)(size_t)sm + G1_R * G1_SLOT ? slot - (G1_R - 1) * G1_SLOT : slot + G1_SLOT;
    if (c == 0) STAMP(p, 1);
    if (c == 1) STAMP(p, 2);
    if (c > 0) {
      asm volatile("s_waitcnt vmcnt(16)" ::: "memory");
      if (c == 1) STAMP(p, 3);
      if (c == 2) STAMP(p, 4);
      __builtin_amdgcn_s_barrier();
    }
  }
  drain();
  __builtin_amdgcn_s_barrier();
  __builtin_amdgcn_s_setprio(0);
}
struct G1Frag { v4u a[4], b[4]; };
DEV void g1_main(const Params& p, Ts& ts, LAS char* sm, rsrc_t rs, int rt, int ct, int w, int lane, v2u tag) {
  const int wf = w & 1, wt = w >> 1, g = lane >> 4, r16 = lane & 15;
  f32x4 acc[4][4] = {};
  const int q4 = r16 >> 2, p4 = lane & 3;
  int aoff[2], boff[2];
#pragma unroll
  for (int i = 0; i < 2; ++i) aoff[i] = 16384 + (8 * g + q4) * 256 + (((8 * wf + 4 * i + (p4 >> 1)) ^ (q4 << 2) ^ ((g & 1) << 1)) << 4) + 8 * (p4 & 1);
  const int tok0 = wt * 64 + r16;
#pragma unroll
  for (int s = 0; s < 2; ++s) boff[s] = tok0 * 128 + (((4 * s + g) ^ ((tok0 >> 1) & 7)) << 4);
  auto rd = [&](int c, int s) {
    G1Frag f;
    LAS char* base = sm + (c % G1_R) * G1_SLOT;
#pragma unroll
    for (int i = 0; i < 4; ++i) {
      const int o = (aoff[i >> 1] ^ ((i & 1) << 5)) + s * 8192;
      const v2u a0 = __builtin_bit_cast(v2u, __builtin_amdgcn_ds_read_tr16_b64_v4i16((LAS s16x4*)(base + o)));
      const v2u a1 = __builtin_bit_cast(v2u, __builtin_amdgcn_ds_read_tr16_b64_v4i16((LAS s16x4*)(base + o + 1024)));
      f.a[i] = (v4u){a0.x, a0.y, a1.x, a1.y};
      f.b[i] = *(const LAS v4u*)(base + boff[s] + 2048 * i);
    }
    return f;
  };
  G1Frag fr[16];
#pragma unroll
  for (int st = -1; st < 16; ++st) {
    const int ld = st + 1;
    if (ld < 16) {
      if ((ld & 1) == 0) lds_barrier();
      if (ld == 0) STAMP(p, 19);
      if (ld == 2) STAMP(p, 20);
      if (ld == 4) STAMP(p, 24);
      if (ld == 8) STAMP(p, 25);
      if (ld == 12) STAMP(p, 26);
      if (ld == 14) STAMP(p, 29);
      fr[ld] = rd(ld >> 1, ld & 1);
    }
    if (st >= 0) {
#pragma unroll
      for (int i = 0; i < 4; ++i)
#pragma unroll
        for (int j = 0; j < 4; ++j)
          acc[i][j] = __builtin_amdgcn_mfma_f32_16x16x32_bf16(__builtin_bit_cast(bf16x8, fr[st].a[i]), __builtin_bit_cast(bf16x8, fr[st].b[j]), acc[i][j], 0, 0, 0);
#pragma unroll
      for (int i = 0; i < 12; ++i) {
        __builtin_amdgcn_sched_group_barrier(0x8, 1, 0);
        __builtin_amdgcn_sched_group_barrier(0x100, 1, 0);
      }
      __builtin_amdgcn_sched_group_barrier(0x8, 4, 0);
    }
    __builtin_amdgcn_sched_barrier(0);
  }
  STAMP(p, 3);
  __builtin_amdgcn_s_setprio(2);
  lds_wait(sm, 8);
  unsigned gs[4], gc[4];
#pragma unroll
  for (int i = 0; i < 4; ++i) {
    gs[i] = *(const LAS unsigned*)(sm + GR + (tok0 + 16 * i) * 4);
    gc[i] = *(const LAS unsigned*)(sm + GR + 512 + (wf * 64 + r16 + 16 * i) * 4);
  }
  STAMP(p, 30);
  float c1[4], sd[4];
#pragma unroll
  for (int j = 0; j < 4; ++j) {
    const unsigned m2 = __builtin_amdgcn_perm(gs[j], gs[j], 0x01000100u) ^ 0x80008000u;
    const v2u bm = {g == 0 ? m2 : 0u, 0u};
#pragma unroll
    for (int i = 0; i < 4; ++i)
      acc[i][j] = __builtin_amdgcn_mfma_f32_16x16x16bf16_1k(__builtin_bit_cast(s16x4, (v2u){gc[i], 0u}), __builtin_bit_cast(s16x4, bm), acc[i][j], 0, 0, 0);
    sd[j] = bfhi(gs[j]);
    c1[j] = __builtin_amdgcn_rcpf(sd[j]) * -1.44269504088896f;
  }
  const int hbase = WS_HF + ((2 * rt + wt) * 56 + 4 * ct + 2 * wf) * 4096 + lane * 16;
#pragma unroll
  for (int m = 0; m < 2; ++m)
#pragma unroll
    for (int j = 0; j < 4; ++j) {
      v4u o;
#pragma unroll
      for (int e = 0; e < 4; ++e) {
        const f32x4 a4 = acc[2 * m + (e >> 1)][j];
        const f32x2 t = (e & 1) ? a4.zw : a4.xy;
        const f32x2 u = t * c1[j];
        const f32x2 ex = {__builtin_amdgcn_exp2f(u.x), __builtin_amdgcn_exp2f(u.y)};
        const f32x2 dn = __builtin_elementwise_fma(ex, (f32x2){sd[j], sd[j]}, (f32x2){sd[j], sd[j]});
        const f32x2 r = t * (f32x2){__builtin_amdgcn_rcpf(dn.x), __builtin_amdgcn_rcpf(dn.y)};
        o[e] = pk_bf16(r.x, r.y);
      }
      __builtin_amdgcn_raw_buffer_store_b128(o, rs, hbase + m * 4096 + j * 1024, 0, SC1);
      __builtin_amdgcn_sched_barrier(0);
    }
  STAMP(p, 4);
  publish(rs, sm, 1, 4, WS_FH + (rt * 16 + ct) * FSTR, tag, lane);
  __builtin_amdgcn_s_setprio(0);
}

DEV int ks_of(int w, int rl, int i) { return 7 * ((w + 2 * rl) & 7) + i; }
template <int LO, int HI> DEV void w2_load(rsrc_t rs, int woff, int w, int rl, v4u (&wr)[28]) {
#pragma unroll
  for (int i = LO; i < HI; ++i) wr[i] = __builtin_amdgcn_raw_buffer_load_b128(rs, woff + (i & 3) * 1024, ks_of(w, rl, i >> 2) * 4096, SC1);
}

DEV void conv(const Params& p, Ts& ts, LAS char* sm, rsrc_t rs, int b, int hw, int lane, v2u tag, int woff, int w, int rl, v4u (&wr)[28]) {
  const int ch = hw >> 1, eh = hw & 1, g = lane >> 4, r16 = lane & 15, d = chan_of(b, ch);
  LAS char* xs = sm + CV_X + ch * CV_XS;
  if (eh == 0 && lane < 34) *(LAS v4u*)(xs + (lane < 32 ? lane : 256 + lane) * 16) = (v4u){0u, 0u, 0u, 0u};
  lds_wait(sm, 3);
  STAMP(p, 14);
  v4u xv[2];
#pragma unroll
  for (int i = 0; i < 2; ++i)
    xv[i] = __builtin_amdgcn_raw_buffer_load_b128(rs, WS_XT + (hw * 64 + i * 32 + (lane >> 1)) * 8192 + (b & 7) * 1024 + (2 * (b >> 3) + (lane & 1)) * 16, 0, SC1);
  if (hw < 2) w2_load<0, W2_EARLY>(rs, woff, w, rl, wr);
  w2_load<W2_EARLY, 28>(rs, woff, w, rl, wr);
  __builtin_amdgcn_sched_barrier(0);
  STAMP(p, 15);
#pragma unroll
  for (int i = 0; i < 2; ++i) *(LAS v4u*)(sm + CV_X + (lane & 1) * CV_XS + 512 + (hw * 64 + i * 32 + (lane >> 1)) * 16) = xv[i];
  kexpand(sm, hw, lane);
  wave_sync(sm, 4, 4);
  STAMP(p, 6);
  const LAS char* ka = sm + KG_TAB + ch * 2 * KG_WAVE + (r16 & 3) * CV_KS + (517 - (r16 >> 2) + 2 * g) * 8;
  const LAS char* xa = xs + 512 + r16 * 32 + g * 16;
  auto lda = [&](const LAS char* ptr) {
    const v2u lo = *(const volatile LAS v2u*)ptr, hi = *(const volatile LAS v2u*)(ptr + 8);
    return __builtin_bit_cast(f16x8, (v4u){lo.x, lo.y, hi.x, hi.y});
  };
  f32x4 acc[8];
#pragma unroll
  for (int a = 0; a < 8; ++a) acc[a] = (f32x4){0.f, 0.f, 0.f, 0.f};
  const LAS char* ke = ka - 256 * eh;
  const LAS char* xe = xa - 256 * eh;
  static_for<0, 4>([&](auto ee) {
    constexpr int e = decltype(ee)::v;
    f16x8 A[8], B[8];
#pragma unroll
    for (int i = 0; i < 8; ++i) { A[i] = lda(ke - 64 * (e + 1) - 512 * i); B[i] = *(const LAS f16x8*)(xe - 64 * (e + 1) + 512 * i); }
#pragma unroll
    for (int a = 0; a < 8; ++a)
#pragma unroll
      for (int dd = 0; dd <= a; ++dd) acc[a] = __builtin_amdgcn_mfma_f32_16x16x32_f16(A[dd], B[a - dd], acc[a], 0, 0, 0);
    __builtin_amdgcn_sched_barrier(0);
  });
  LAS f32x4* ex = (LAS f32x4*)(sm + CV_E);
  f32x4 own[4];
#pragma unroll
  for (int i = 0; i < 4; ++i) {
    ex[(hw * 4 + i) * 64 + lane] = eh ? acc[i] : acc[4 + i];
    own[i] = eh ? acc[4 + i] : acc[i];
  }
  {
    const f16x8 A0 = lda(ka);
#pragma unroll
    for (int i = 0; i < 4; ++i) own[i] = __builtin_amdgcn_mfma_f32_16x16x32_f16(A0, *(const LAS f16x8*)(xa + 512 * (4 * eh + i)), own[i], 0, 0, 0);
  }
  STAMP(p, 7);
  wave_sync(sm, 6 + ch, 2);
  const f32x2 md = *(const LAS f32x2*)(sm + SY + 48 + ch * 8);
  const float mult = md.x, dsk = md.y;
#pragma unroll
  for (int i = 0; i < 4; ++i) {
    const f32x4 y = (own[i] + ex[((hw ^ 1) * 4 + i) * 64 + lane]) * mult;
    const int t0 = 256 * (4 * eh + i) + 16 * r16 + 4 * g;
    const v2u xr = *(const LAS v2u*)(xs + 512 + t0 * 2);
    const unsigned xr0 = xr.x, xr1 = xr.y;
    const f16x2 x0 = __builtin_bit_cast(f16x2, xr0), x1 = __builtin_bit_cast(f16x2, xr1);
    const unsigned y01 = pk_bf16(y[0], y[1]), y23 = pk_bf16(y[2], y[3]);
    const unsigned d01 = pk_bf16((float)x0.x * dsk, (float)x0.y * dsk), d23 = pk_bf16((float)x1.x * dsk, (float)x1.y * dsk);
    const v2u o = {pk_bf16(bflo(y01) + bflo(d01), bfhi(y01) + bfhi(d01)), pk_bf16(bflo(y23) + bflo(d23), bfhi(y23) + bfhi(d23))};
    __builtin_amdgcn_raw_buffer_store_b64(o, rs, WS_ST + d * 4096 + t0 * 2, 0, SC1);
  }
  publish(rs, sm, 2, 4, WS_FS + b * FSTR, tag, lane);
}

DEV void gemm2(const Params& p, Ts& ts, LAS char* sm, rsrc_t rs, int rt2, int ct2, int rl, bool isL, int w, int lane, v2u tag, v4u (&wr)[28]) {
  const int g = lane >> 4, hb = (lane >> 3) & 1, cb = lane & 7;
  if (w == 0) { wait_flags(rs, WS_FH + (rt2 >> 1) * 16 * FSTR, 14, tag, lane); lds_set(sm, 5); }
  else lds_wait(sm, 5);
  STAMP(p, 10);
  v4u ha[28];
  const int hoff = WS_HF + rt2 * 56 * 4096 + lane * 16;
  auto hload = [&](int i) { return __builtin_amdgcn_raw_buffer_load_b128(rs, hoff + (i & 3) * 1024, ks_of(w, rl, i >> 2) * 4096, SC1); };
#pragma unroll
  for (int i = 0; i < 16; ++i) ha[i] = hload(i);
  __builtin_amdgcn_sched_barrier(0);
  f32x4 acc[4][4] = {};
  auto mm = [&](int ks, int mt) {
#pragma unroll
    for (int t = 0; t < 4; ++t)
      acc[mt][t] = __builtin_amdgcn_mfma_f32_16x16x32_bf16(__builtin_bit_cast(bf16x8, ha[4 * ks + mt]), __builtin_bit_cast(bf16x8, wr[4 * ks + t]), acc[mt][t], 0, 0, 0);
  };
#pragma unroll
  for (int ks = 0; ks < 4; ++ks) {
#pragma unroll
    for (int mt = 0; mt < 4; ++mt) mm(ks, mt);
    __builtin_amdgcn_sched_barrier(0);
    if (ks < 2) {
#pragma unroll
      for (int i = 0; i < 6; ++i) { const int e = 6 * ks + i; ha[16 + 4 * (e % 3) + e / 3] = hload(16 + 4 * (e % 3) + e / 3); }
    }
    __builtin_amdgcn_sched_barrier(0);
  }
  lds_wait(sm, w < 4 ? 1 : 2, w < 4 && isL ? 0u : 4u);
  LAS f32x4* red = (LAS f32x4*)(sm + (w < 4 ? 32768 + w * 16384 : (w < 6 ? (w - 4) * 16384 : 98304 + (w - 6) * 16384)));
#pragma unroll
  for (int mt = 0; mt < 4; ++mt) {
#pragma unroll
    for (int ks = 4; ks < 7; ++ks) mm(ks, mt);
    if (mt > 0) {
#pragma unroll
      for (int t = 0; t < 4; ++t) red[((mt - 1) * 4 + t) * 64 + lane] = acc[mt - 1][t];
    }
#pragma unroll
    for (int i = 0; i < 4; ++i) { __builtin_amdgcn_sched_group_barrier(0x8, 3, 0); __builtin_amdgcn_sched_group_barrier(0x200, 1, 0); }
    __builtin_amdgcn_sched_barrier(0);
  }
#pragma unroll
  for (int t = 0; t < 4; ++t) red[(12 + t) * 64 + lane] = acc[3][t];
  STAMP(p, 11);
  const int tok = rt2 * 64 + 16 * w + 4 * g, chn = ct2 * 64 + 8 * cb + 4 * hb;
  v2u xv[4];
  f32x4 o[4];
  if (w < 4) {
#pragma unroll
    for (int i = 0; i < 4; ++i) xv[i] = *(const GAS v2u*)((const GAS char*)p.x + (size_t)(tok + i) * 1024 + chn * 2);
    wave_sync(sm, 10, 4);
#pragma unroll
    for (int t = 0; t < 4; ++t) {
      const LAS f32x4* r0 = (const LAS f32x4*)(sm + 32768) + (w * 4 + t) * 64 + lane;
      o[t] = (r0[0] + r0[1024]) + (r0[2048] + r0[3072]);
    }
    if (w == 0) {
      lds_wait(sm, 2, 4u);
      __builtin_amdgcn_s_sleep(16);
      wait_flags(rs, WS_FS + ct2 * 32 * FSTR, 32, tag, lane);
      lds_set(sm, 9);
    } else lds_wait(sm, 9);
#pragma unroll
    for (int i = 0; i < 2; ++i) {
      const int c = 16 * w + 8 * i + (lane >> 3);
      *(LAS v4u*)(sm + G2_S + c * 144 + (lane & 7) * 16) =
          __builtin_amdgcn_raw_buffer_load_b128(rs, WS_ST + (ct2 * 64 + c) * 4096 + rt2 * 128 + (lane & 7) * 16, 0, SC1);
    }
  }
  STAMP(p, 28);
  lds_barrier();
  STAMP(p, 12);
  if (w < 4) {
#pragma unroll
    for (int t = 0; t < 4; ++t) {
      const LAS f32x4* r0 = (const LAS f32x4*)sm + (w * 4 + t) * 64 + lane;
      o[t] += (r0[0] + r0[1024]) + (r0[6144] + r0[7168]);
    }
    v2u sv[4];
#pragma unroll
    for (int u = 0; u < 4; ++u) sv[u] = *(const LAS v2u*)(sm + G2_S + (8 * cb + 4 * hb + u) * 144 + 32 * w + 8 * g);
#pragma unroll
    for (int i = 0; i < 4; ++i) {
      v2u res;
#pragma unroll
      for (int u = 0; u < 2; ++u) {
        const unsigned ob = pk_bf16(o[2 * u][i], o[2 * u + 1][i]);
        const unsigned gb = pk_bf16(silu_f(bflo(ob)), silu_f(bfhi(ob)));
        const unsigned s0 = sv[2 * u][i >> 1], s1 = sv[2 * u + 1][i >> 1];
        const float sa = (i & 1) ? bfhi(s0) : bflo(s0), sb = (i & 1) ? bfhi(s1) : bflo(s1);
        const unsigned pr = pk_bf16(sa * bflo(gb), sb * bfhi(gb));
        const unsigned xx = xv[i][u];
        res[u] = pk_bf16(bflo(xx) + bflo(pr), bfhi(xx) + bfhi(pr));
      }
      *(GAS v2u*)((GAS char*)p.out + (size_t)(tok + i) * 1024 + chn * 2) = res;
    }
  }
  STAMP(p, 13);
}

__global__ void __launch_bounds__(512, 2) fused_kernel(Params p) {
  __shared__ __attribute__((aligned(16))) char smem[LDS_BYTES];
  LAS char* sm = (LAS char*)smem;
  const int tid = threadIdx.x, lane = tid & 63, w = __builtin_amdgcn_readfirstlane(tid >> 6);
  const int b = blockIdx.x, xcd = b & 7, idx = b >> 3;
  const bool isL = idx < 4;
  const int ii = idx - 4, rt = (xcd & 3) * 4 + (ii & 3), ct = (xcd >> 2) * 7 + (ii >> 2);
  const int rl = idx >> 3, rt2 = (xcd & 3) * 8 + (xcd >> 2) * 4 + rl, ct2 = idx & 7;
  const int t8 = (xcd & 3) * 64 + (xcd >> 2) * 32 + idx;
  Ts ts = {};
  STAMP(p, 0);
  const rsrc_t rs = __builtin_amdgcn_make_buffer_rsrc(p.ws, 0, WS_BYTES, 0x00020000);
  asm volatile("" ::"s"(p.x), "s"(p.lar), "s"(p.aim), "s"(p.cre), "s"(p.cim), "s"(p.ldt), "s"(p.dsk), "s"(p.w1), "s"(p.w2), "s"(p.ws));
  v2u ep;
  KPar kp;
  v4u early[8];
  unsigned pf;
  asm volatile("global_load_dwordx2 %0, %1, %2 sc1" : "=v"(ep) : "v"(WS_EPOCH), "s"(p.ws));
  if (w < 4) {
    const unsigned d2 = chan_of(b, w >> 1) * 2, o = d2 * NM + ((w & 1) * 64 + lane) * 2, o2 = o ^ 128;
    asm volatile("global_load_ushort %0, %8, %11\n\tglobal_load_ushort %1, %9, %12\n\tglobal_load_ushort %2, %9, %13\n\t"
                 "global_load_ushort %3, %9, %14\n\tglobal_load_ushort %4, %9, %15\n\tglobal_load_ushort %5, %10, %14\n\t"
                 "global_load_ushort %6, %10, %15\n\tglobal_load_ushort %7, %8, %16"
                 : "=&v"(kp.ldt), "=&v"(kp.lar), "=&v"(kp.aim), "=&v"(kp.c0), "=&v"(kp.c1), "=&v"(kp.c2), "=&v"(kp.c3), "=&v"(kp.dsk)
                 : "v"(d2), "v"(o), "v"(o2), "s"(p.ldt), "s"(p.lar), "s"(p.aim), "s"(p.cre), "s"(p.cim), "s"(p.dsk));
  }
  if (w >= 6) {
    const int piece = isL ? 0 : (b - 32) * 2;
    const char* sb = w == 6 ? (const char*)p.x + (size_t)t8 * 8192 : (const char*)p.w2 + (size_t)(piece % 56) * 32768 + (piece / 56) * 128;
    const unsigned o = w == 6 ? lane * 16 : (16 * ((lane >> 3) & 1) + 4 * (lane >> 4)) * 1024 + (lane & 7) * 16;
    const unsigned o2 = o + (w == 6 ? 4096 : 32768);
    asm volatile("global_load_dwordx4 %0, %8, %10\n\tglobal_load_dwordx4 %1, %8, %10 offset:1024\n\tglobal_load_dwordx4 %2, %8, %10 offset:2048\n\t"
                 "global_load_dwordx4 %3, %8, %10 offset:3072\n\tglobal_load_dwordx4 %4, %9, %10\n\tglobal_load_dwordx4 %5, %9, %10 offset:1024\n\t"
                 "global_load_dwordx4 %6, %9, %10 offset:2048\n\tglobal_load_dwordx4 %7, %9, %10 offset:3072"
                 : "=&v"(early[0]), "=&v"(early[1]), "=&v"(early[2]), "=&v"(early[3]), "=&v"(early[4]), "=&v"(early[5]), "=&v"(early[6]), "=&v"(early[7])
                 : "v"(o), "v"(o2), "s"(sb));
  }
  if ((w & 6) == 4) {
    const u64 pc = __builtin_amdgcn_s_getpc() & ~(u64)255;
    asm volatile("global_load_dword %0, %1, %2" : "=v"(pf) : "v"(min(idx * 1024 + (w - 4) * 512 + (lane & 3) * 128, CODE_BYTES - 128)), "s"(pc));
    if (isL) {
      drain();
#pragma unroll 1
      for (int i = 0; i < 8; ++i) bar();
    } else g1_loader(p, ts, sm, rt, ct, 2 * (ii & 3) + 2 * (ii >> 2), w - 4, lane);
    asm volatile("" ::"v"(pf));
  }
  __builtin_amdgcn_sched_barrier(0);
  const int woff = WS_W2 + ct2 * 56 * 4096 + lane * 16;
  v4u wr[28];
  auto make_tag = [&]() {
    unsigned lo, hi;
    asm volatile("v_readfirstlane_b32 %0, %2\n\tv_readfirstlane_b32 %1, %3" : "=s"(lo), "=s"(hi) : "v"(ep.x), "v"(ep.y));
    const u64 t = (((u64)hi << 32) | lo) + 1ull;
    return (v2u){(unsigned)t, (unsigned)(t >> 32)};
  };
  v2u tag;
  if (w == 4 || w == 5) {
    tag = make_tag();
  } else if (w < 4) {
    v4u cw[16];
    const int cg = (xcd & 3) * 4 + idx;
    if (isL) cs_load(p, (xcd >> 2) * 14 + min(cg, 13), w, lane, cw);
    if (w == 0 && lane < 12) *(LAS unsigned*)(sm + SY + lane * 4) = 0u;
    if (w == 1 && lane < 8) *(LAS unsigned*)(sm + KG_K + (lane >> 2) * KG_KCH + (lane & 3) * 4) = 0u;
    __builtin_amdgcn_sched_barrier(0);
    drain();
    __builtin_amdgcn_sched_barrier(0);
    asm volatile("" : "+v"(kp.ldt), "+v"(kp.lar), "+v"(kp.aim), "+v"(kp.c0), "+v"(kp.c1), "+v"(kp.c2), "+v"(kp.c3), "+v"(kp.dsk));
    tag = make_tag();
    STAMP(p, 1);
    if (isL) cs_role(sm, rs, (xcd >> 2) * 14 + cg, cg < 14, w, lane, tag, cw);
    STAMP(p, 16);
    kgen(p, ts, sm, kp, w, lane);
    STAMP(p, 2);
    if (isL) {
      asm volatile("s_waitcnt lgkmcnt(0)" ::: "memory");
#pragma unroll 1
      for (int i = 0; i < 7; ++i) bar();
    } else g1_main(p, ts, sm, rs, rt, ct, w, lane, tag);
    STAMP(p, 5);
    w2_load<0, 28>(rs, woff, w, rl, wr);
    __builtin_amdgcn_sched_barrier(0);
    STAMP(p, 23);
  } else {
    drain();
    __builtin_amdgcn_sched_barrier(0);
    asm volatile("" : "+v"(early[0]), "+v"(early[1]), "+v"(early[2]), "+v"(early[3]), "+v"(early[4]), "+v"(early[5]), "+v"(early[6]), "+v"(early[7]));
    tag = make_tag();
    STAMP(p, 1);
    if (w == 6) ln_wave(p, ts, rs, t8, lane, tag, early);
    else if (!isL) { w2_piece_store(rs, (b - 32) * 2, lane, early); w2_piece_store(rs, (b - 32) * 2 + 1, lane, early + 4); }
    STAMP(p, 27);
    bar();
    if (!isL) bar();
    drain();
    if (lane == 0 && (w == 6 || !isL)) __builtin_amdgcn_raw_buffer_store_b64(tag, rs, w == 6 ? WS_FX + b * FSTR : WS_FW + (b - 32) * FSTR, 0, SC1);
    STAMP(p, 22);
    if (isL) bar();
    bar(); bar(); bar();
    if (w == 7) {
      const int l28 = lane < 28 ? lane : (isL ? min(lane, 55) + 84 : 27);
      bool first = true;
#pragma unroll 1
      for (unsigned spins = 0; spins < SPIN_CAP; ++spins) {
        asm volatile("" ::: "memory");
        const v2u f = __builtin_amdgcn_raw_buffer_load_b64(rs, WS_FW + (ct2 * 28 + l28) * FSTR, 0, SC1);
        __builtin_amdgcn_sched_barrier(0);
        if (first) { bar(); first = false; }
        if (__all(f.x == tag.x && f.y == tag.y)) break;
        __builtin_amdgcn_s_sleep(8);
      }
    } else bar();
    bar();
    const int poff = w == 7 ? WS_FX + lane * FSTR : WS_GS + (isL ? 0 : rt * 1024) + lane * 8;
    const int pst1 = w == 7 ? 64 * FSTR : 512, pst2 = w == 7 ? 128 * FSTR : WS_GC - WS_GS + (isL ? 0 : (ct - rt) * 1024);
    const unsigned tx = w == 7 ? tag.y : tag.x, mx = w == 7 ? ~0u : 0u;
    v2u f[4];
#pragma unroll
    for (int i = 0; i < 4; ++i) f[i] = __builtin_amdgcn_raw_buffer_load_b64(rs, poff, (i & 1) * pst1 + (i >> 1) * pst2, SC1);
    w2_load<0, W2_EARLY>(rs, woff, w, rl, wr);
    __builtin_amdgcn_sched_barrier(0);
    bar();
#pragma unroll 1
    for (unsigned spins = 0; spins < SPIN_CAP; ++spins) {
      unsigned bad = 0;
#pragma unroll
      for (int i = 0; i < 4; ++i) bad |= (f[i].y ^ tx) | ((f[i].x ^ tag.x) & mx);
      if (__all(bad == 0u)) break;
      __builtin_amdgcn_s_sleep(8);
      asm volatile("" ::: "memory");
#pragma unroll
      for (int i = 0; i < 4; ++i) f[i] = __builtin_amdgcn_raw_buffer_load_b64(rs, poff, (i & 1) * pst1 + (i >> 1) * pst2, SC1);
    }
    if (w == 6) {
#pragma unroll
      for (int i = 0; i < 4; ++i) *(LAS unsigned*)(sm + GR + i * 256 + lane * 4) = f[i].x;
      asm volatile("" ::: "memory");
    }
    lds_set(sm, w == 6 ? 8 : 3);
    if (b == 0 && w == 7 && lane == 0) __builtin_amdgcn_raw_buffer_store_b64(tag, rs, WS_EPOCH, 0, SC1);
  }
  if (w >= 4) {
    STAMP(p, 9);
    conv(p, ts, sm, rs, b, w - 4, lane, tag, woff, w, rl, wr);
    STAMP(p, 8);
  }
  gemm2(p, ts, sm, rs, rt2, ct2, rl, isL, w, lane, tag, wr);
  {
    unsigned until = 4000;
    if (b == PROBE_B && w == PROBE_W) until += 4 * (ts.t[PROBE] - ts.t[0]);
    while ((unsigned)__builtin_amdgcn_s_memrealtime() - ts.t[0] < until) __builtin_amdgcn_s_sleep(2);
  }
}

extern "C" void kernel_launch(void* const* d_in, const int* in_sizes, int n_in, void* d_out, int out_size, void* d_ws, size_t ws_size,
                              hipStream_t stream) {
  Params p;
  p.x = (const u16*)d_in[0]; p.lar = (const u16*)d_in[1]; p.aim = (const u16*)d_in[2]; p.cre = (const u16*)d_in[3];
  p.cim = (const u16*)d_in[4]; p.ldt = (const u16*)d_in[5]; p.dsk = (const u16*)d_in[6]; p.w1 = (const u16*)d_in[8];
  p.w2 = (const u16*)d_in[9]; p.out = (u16*)d_out; p.ws = (char*)d_ws;
  fused_kernel<<<256, 512, 0, stream>>>(p);
}
```

```cpp
#define STAMPS
#define PROBE 0
#define PV 0
#include <hip/hip_runtime.h>
#include <hip/hip_bf16.h>
#include <stdint.h>

typedef unsigned short u16;
typedef unsigned long long u64;
typedef unsigned v4u __attribute__((ext_vector_type(4)));
typedef unsigned v2u __attribute__((ext_vector_type(2)));
typedef float f32x2 __attribute__((ext_vector_type(2)));
typedef float f32x4 __attribute__((ext_vector_type(4)));
typedef float f32x16 __attribute__((ext_vector_type(16)));
typedef short s16x4 __attribute__((ext_vector_type(4)));
typedef __bf16 bf16x2 __attribute__((ext_vector_type(2)));
typedef __bf16 bf16x8 __attribute__((ext_vector_type(8)));
typedef _Float16 f16x2 __attribute__((ext_vector_type(2)));
typedef _Float16 f16x8 __attribute__((ext_vector_type(8)));
typedef __amdgpu_buffer_rsrc_t rsrc_t;
#define GAS __attribute__((address_space(1)))
#define LAS __attribute__((address_space(3)))
#define DEV __device__ __forceinline__

constexpr int L = 2048, D = 512, NM = 128, F = 1792;
constexpr int WS_EPOCH = 0;
constexpr int FSTR = 64;
constexpr int WS_FH = 4096;
constexpr int WS_FX = WS_FH + 256 * FSTR;
constexpr int WS_FW = WS_FX + 256 * FSTR;
constexpr int WS_FS = WS_FW + 256 * FSTR;
constexpr int WS_GS = WS_FS + 256 * FSTR;
constexpr int WS_GC = WS_GS + L * 8;
constexpr int WS_XT = 1 << 17;
constexpr int WS_ST = WS_XT + (2 << 20);
constexpr int WS_W2 = WS_ST + (2 << 20);
constexpr int WS_HF = WS_W2 + (2 << 20);
constexpr int WS_BYTES = WS_HF + L * F * 2;
#define LOADER_PRIO 0
#define LOADER_DELAY 10
constexpr int SC1 = 16;
constexpr int W2_EARLY = 10;
constexpr int CODE_BYTES = 27 * 1024;
constexpr unsigned SPIN_CAP = 1u << 16;
constexpr int G1_SLOT = 32768, G1_R = 3;
constexpr int KG_ROW = 272;
constexpr int KG_WAVE = 53 * KG_ROW;
constexpr int KG_TAB = G1_R * G1_SLOT;
constexpr int CV_KS = 4288;
constexpr int KG_K = KG_TAB + 4 * KG_WAVE;
constexpr int KG_KCH = 676 * 4;
constexpr int SY = KG_K + 2 * KG_KCH;
constexpr int GR = SY + 64;
constexpr int LDS_USED = GR + 1024;
constexpr int G2_S = 144384;
constexpr int CV_X = 0;
constexpr int CV_XS = (256 + 2048 + 16) * 2;
constexpr int CV_E = 2 * CV_XS;
constexpr int LDS_BYTES = 160 * 1024;
static_assert(WS_GC + F * 8 <= WS_XT && G2_S >= KG_TAB + 2 * KG_WAVE + 4 * CV_KS && G2_S + 64 * 144 <= KG_K, "maps");
static_assert(4 * CV_KS <= 2 * KG_WAVE && (2 * KG_WAVE) % 16 == 0 && LDS_USED <= LDS_BYTES && CV_E % 16 == 0 && CV_E + 16384 <= G1_SLOT, "lds map");

DEV float bf2f(unsigned v) { return __uint_as_float(v << 16); }
DEV float bflo(unsigned v) { return __uint_as_float(v << 16); }
DEV float bfhi(unsigned v) { return __uint_as_float(v & 0xffff0000u); }
DEV unsigned pk_bf16(float a, float b) { f32x2 f = {a, b}; return __builtin_bit_cast(unsigned, __builtin_convertvector(f, bf16x2)); }
DEV unsigned pk_f16(float a, float b) { f32x2 f = {a, b}; return __builtin_bit_cast(unsigned, __builtin_convertvector(f, f16x2)); }
DEV float rbf(float a) { return bflo(pk_bf16(a, a) & 0xffffu); }
DEV float dot2bf(unsigned a, unsigned b, float c) {
  return __builtin_amdgcn_fdot2_f32_bf16(__builtin_bit_cast(bf16x2, a), __builtin_bit_cast(bf16x2, b), c, false);
}
DEV float silu_f(float v) { return v * __builtin_amdgcn_rcpf(1.f + __builtin_amdgcn_exp2f(v * -1.44269504088896f)); }
DEV void lds_barrier() { asm volatile("s_waitcnt lgkmcnt(0)" ::: "memory"); __builtin_amdgcn_s_barrier(); asm volatile("" ::: "memory"); }
DEV void drain() { asm volatile("s_waitcnt vmcnt(0)" ::: "memory"); }
template <int CTRL> DEV float dpp_add(float v) {
  return v + __builtin_bit_cast(float, __builtin_amdgcn_update_dpp(0, __builtin_bit_cast(int, v), CTRL, 0xf, 0xf, true));
}
DEV float row16_sum(float v) { v = dpp_add<0xb1>(v); v = dpp_add<0x4e>(v); v = dpp_add<0x124>(v); return dpp_add<0x128>(v); }
DEV float swap32_add(float a, float b) {
  auto r = __builtin_amdgcn_permlane32_swap(__float_as_uint(a), __float_as_uint(b), false, false);
  return __uint_as_float(r[0]) + __uint_as_float(r[1]);
}
DEV float swap16_add(float a, float b) {
  auto r = __builtin_amdgcn_permlane16_swap(__float_as_uint(a), __float_as_uint(b), false, false);
  return __uint_as_float(r[0]) + __uint_as_float(r[1]);
}
DEV float rdlane(float v, int l) { return __uint_as_float(__builtin_amdgcn_readlane(__float_as_uint(v), l)); }
template <int I> struct IC { static constexpr int v = I; };
template <int B, int E, class Fn> DEV void static_for(Fn&& f) { if constexpr (B < E) { f(IC<B>{}); static_for<B + 1, E>(f); } }

struct Ts { unsigned t[32]; };
#define STAMP(p, i) do { __builtin_amdgcn_sched_barrier(0); ts.t[i] = (unsigned)__builtin_amdgcn_s_memrealtime(); __builtin_amdgcn_sched_barrier(0); } while (0)

struct Params {
  const u16 *x, *lar, *aim, *cre, *cim, *ldt, *dsk, *w1, *w2;
  u16* out;
  char* ws;
};

DEV void wait_flags(rsrc_t rs, int off, int n, v2u tag, int lane) {
#pragma unroll 1
  for (unsigned spins = 0; spins < SPIN_CAP; ++spins) {
    asm volatile("" ::: "memory");
    const v2u v = __builtin_amdgcn_raw_buffer_load_b64(rs, off + min(lane, n - 1) * FSTR, 0, SC1);
    if (__all(v.x == tag.x && v.y == tag.y)) break;
    __builtin_amdgcn_s_sleep(4);
  }
  __builtin_amdgcn_fence(__ATOMIC_ACQUIRE, "wavefront");
}
DEV void publish(rsrc_t rs, LAS char* sm, int word, unsigned nwaves, int off, v2u tag, int lane) {
  drain();
  if (lane == 0) {
    const unsigned old = __hip_atomic_fetch_add((LAS unsigned*)(sm + SY) + word, 1u, __ATOMIC_RELAXED, __HIP_MEMORY_SCOPE_WORKGROUP);
    if (old == nwaves - 1) __builtin_amdgcn_raw_buffer_store_b64(tag, rs, off, 0, SC1);
  }
}
DEV void wave_sync(LAS char* sm, int word, unsigned n) {
  asm volatile("s_waitcnt lgkmcnt(0)" ::: "memory");
  volatile LAS unsigned* c = (volatile LAS unsigned*)(sm + SY) + word;
  if ((threadIdx.x & 63) == 0) __hip_atomic_fetch_add((LAS unsigned*)c, 1u, __ATOMIC_RELAXED, __HIP_MEMORY_SCOPE_WORKGROUP);
#pragma unroll 1
  for (unsigned spins = 0; spins < 16 * SPIN_CAP && __builtin_amdgcn_readfirstlane(*c) != n; ++spins) {}
  asm volatile("" ::: "memory");
}

DEV void lds_set(LAS char* sm, int word) { *((volatile LAS unsigned*)(sm + SY) + word) = 1u; }
DEV void lds_wait(LAS char* sm, int word, unsigned want = 1u) {
  volatile LAS unsigned* c = (volatile LAS unsigned*)(sm + SY) + word;
#pragma unroll 1
  for (unsigned spins = 0; spins < 16 * SPIN_CAP && __builtin_amdgcn_readfirstlane(*c) != want; ++spins) __builtin_amdgcn_s_sleep(1);
  __builtin_amdgcn_fence(__ATOMIC_ACQUIRE, "wavefront");
  asm volatile("" ::: "memory");
}
DEV void bar() { asm volatile("" ::: "memory"); __builtin_amdgcn_s_barrier(); asm volatile("" ::: "memory"); }

DEV void ln_wave(const Params& p, Ts& ts, rsrc_t rs, int t8, int lane, v2u tag, const v4u (&xr)[8]) {
  float s[8], q[8], t[4], rz[2], mz[2];
#pragma unroll
  for (int r = 0; r < 8; ++r) {
    s[r] = 0.f; q[r] = 0.f;
#pragma unroll
    for (int j = 0; j < 4; ++j) { s[r] = dot2bf(xr[r][j], 0x3f803f80u, s[r]); q[r] = dot2bf(xr[r][j], xr[r][j], q[r]); }
  }
#pragma unroll
  for (int j = 0; j < 4; ++j) { t[j] = swap32_add(s[j], s[j + 4]); q[j] = swap32_add(q[j], q[j + 4]); }
#pragma unroll
  for (int u = 0; u < 2; ++u) {
    const float mean = row16_sum(swap16_add(t[u], t[u + 2])) * (1.f / D);
    const float var = rbf(fmaxf(row16_sum(swap16_add(q[u], q[u + 2])) * (1.f / D) - mean * mean, 0.f));
    const unsigned ms = pk_bf16(mean, __builtin_amdgcn_sqrtf(rbf(var + 1e-5f)));
    if ((lane & 15) == 0) __builtin_amdgcn_raw_buffer_store_b64((v2u){ms, tag.x}, rs, WS_GS + (t8 * 8 + u + 2 * (lane >> 4)) * 8, 0, SC1);
    rz[u] = __builtin_amdgcn_rcpf(bfhi(ms));
    mz[u] = -bflo(ms) * rz[u];
  }
  float rr[8], mm[8];
#pragma unroll
  for (int r = 0; r < 8; ++r) { rr[r] = rdlane(rz[r & 1], 16 * (r >> 1)); mm[r] = rdlane(mz[r & 1], 16 * (r >> 1)); }
  STAMP(p, 21);
#pragma unroll
  for (int j = 0; j < 4; ++j) {
    __builtin_amdgcn_sched_barrier(0);
    f32x2 v[8];
#pragma unroll
    for (int r = 0; r < 8; ++r) v[r] = __builtin_elementwise_fma((f32x2){bflo(xr[r][j]), bfhi(xr[r][j])}, (f32x2){rr[r], rr[r]}, (f32x2){mm[r], mm[r]});
#pragma unroll
    for (int e = 0; e < 2; ++e) {
      v4u o;
#pragma unroll
      for (int i = 0; i < 4; ++i) o[i] = pk_f16(v[2 * i][e], v[2 * i + 1][e]);
      __builtin_amdgcn_raw_buffer_store_b128(o, rs, WS_XT + t8 * 8192 + (2 * j + e) * 1024 + lane * 16, 0, SC1);
    }
  }
}

DEV void cs_load(const Params& p, int cb, int w, int lane, v4u (&wr)[16]) {
#pragma unroll
  for (int i = 0; i < 16; ++i) wr[i] = *(const GAS v4u*)((const GAS char*)p.w1 + (size_t)(w * 128 + i * 8 + (lane >> 3)) * (F * 2) + cb * 128 + (lane & 7) * 16);
}
DEV void cs_role(LAS char* sm, rsrc_t rs, int tb, bool act, int w, int lane, v2u tag, const v4u (&wr)[16]) {
  unsigned sel_lo = 0x00003f80u, sel_hi = 0x3f800000u;
  asm volatile("" : "+s"(sel_lo), "+s"(sel_hi));
  float c[8] = {0.f, 0.f, 0.f, 0.f, 0.f, 0.f, 0.f, 0.f}, t[4], y[2];
#pragma unroll
  for (int i = 0; i < 16; ++i)
#pragma unroll
    for (int j = 0; j < 4; ++j) { c[2 * j] = dot2bf(sel_lo, wr[i][j], c[2 * j]); c[2 * j + 1] = dot2bf(sel_hi, wr[i][j], c[2 * j + 1]); }
#pragma unroll
  for (int j = 0; j < 4; ++j) t[j] = swap32_add(c[j], c[j + 4]);
#pragma unroll
  for (int u = 0; u < 2; ++u) y[u] = dpp_add<0x128>(swap16_add(t[u], t[u + 2]));
  if ((lane & 8) == 0) *(LAS f32x2*)(sm + 65536 + (w * 64 + (lane & 7) * 8 + 2 * (lane >> 4)) * 4) = (f32x2){y[0], y[1]};
  lds_barrier();
  if (w == 0 && act) {
    float cs = 0.f;
#pragma unroll
    for (int ww = 0; ww < 4; ++ww) cs += *(const LAS float*)(sm + 65536 + (ww * 64 + lane) * 4);
    const float hi = rbf(cs);
    __builtin_amdgcn_raw_buffer_store_b64((v2u){pk_bf16(hi, cs - hi), tag.x}, rs, WS_GC + (tb * 64 + lane) * 8, 0, SC1);
  }
}

DEV void w2_piece_store(rsrc_t rs, int piece, int lane, const v4u* wr) {
  unsigned P[4][2], Q[4][2];
#pragma unroll
  for (int i = 0; i < 4; ++i)
#pragma unroll
    for (int u = 0; u < 2; ++u) {
      const unsigned X = wr[i][u], Y = wr[i][2 + u];
      P[i][u] = __builtin_amdgcn_update_dpp(X, Y, 0x128, 0xf, 0xc, false);
      Q[i][u] = __builtin_amdgcn_update_dpp(Y, X, 0x128, 0xf, 0x3, false);
    }
#pragma unroll
  for (int t = 0; t < 4; ++t) {
    const unsigned sel = (t & 1) ? 0x07060302u : 0x05040100u;
    const int u = t >> 1;
    const v4u B = {__builtin_amdgcn_perm(P[1][u], P[0][u], sel), __builtin_amdgcn_perm(P[3][u], P[2][u], sel),
                   __builtin_amdgcn_perm(Q[1][u], Q[0][u], sel), __builtin_amdgcn_perm(Q[3][u], Q[2][u], sel)};
    __builtin_amdgcn_raw_buffer_store_b128(B, rs, WS_W2 + piece * 4096 + t * 1024 + lane * 16, 0, SC1);
  }
}

struct KPar { unsigned ldt, lar, aim, c0, c1, c2, c3, dsk; };
DEV int chan_of(int b, int ch) { return 16 * (b >> 3) + 8 * ch + (b & 7); }
DEV f32x2 crot(f32x2 w) { return (f32x2){-w.y, w.x}; }
DEV f32x2 cmul(f32x2 a, f32x2 w, f32x2 wr) { return __builtin_elementwise_fma(a.yy, wr, a.xx * w); }
DEV f32x2 cexp_at(float a2, float rv, float pos) {
  const float e = __builtin_amdgcn_exp2f(a2 * pos);
  const float ph = __builtin_amdgcn_fractf(rv * pos);
  return (f32x2){__builtin_amdgcn_cosf(ph), __builtin_amdgcn_sinf(ph)} * e;
}
DEV float exp_acc(float x) {
  const float t = x * 1.44269504088896f;
  const float r = fmaf(x, 1.92596299112661e-8f, fmaf(x, 1.44269504088896f, -t));
  const float e = __builtin_amdgcn_exp2f(t);
  return fmaf(e, r * 0.693147180559945f, e);
}
DEV void kgen(const Params& p, Ts& ts, LAS char* sm, const KPar& k, int w, int lane) {
  const int ch = w >> 1;
  const float dt = rbf(exp_acc(bf2f(k.ldt)));
  const float a2 = -rbf(exp_acc(bf2f(k.lar))) * dt * 1.44269504088896f;
  const float rv = bf2f(k.aim) * dt * 0.159154943091895f;
  LAS char* t = sm + KG_TAB + w * KG_WAVE + lane * 4;
  auto put = [&](int row, f32x2 u) { *(LAS unsigned*)(t + row * KG_ROW) = pk_f16(u.x, u.y); };
  {
    const f32x2 w8 = cexp_at(a2, -rv, 8.f), w8r = crot(w8);
    const f32x2 w64 = cexp_at(a2, -rv, 64.f), w64r = crot(w64), w128 = cmul(w64, w64, w64r);
    f32x2 v[4] = {{1.f, 0.f}, w64, w128, cmul(w128, w64, w64r)};
#pragma unroll
    for (int i = 0; i < 8; ++i)
#pragma unroll
      for (int c = 0; c < 4; ++c) { put(21 + 8 * c + i, v[c]); if (i < 7) v[c] = cmul(v[c], w8, w8r); }
  }
  {
    unsigned mx = max(max(k.c0 & 0x7fffu, k.c1 & 0x7fffu), max(k.c2 & 0x7fffu, k.c3 & 0x7fffu));
    mx = max(mx, (unsigned)__builtin_amdgcn_update_dpp(0, (int)mx, 0xb1, 0xf, 0xf, true));
    mx = max(mx, (unsigned)__builtin_amdgcn_update_dpp(0, (int)mx, 0x4e, 0xf, 0xf, true));
    mx = max(mx, (unsigned)__builtin_amdgcn_update_dpp(0, (int)mx, 0x124, 0xf, 0xf, true));
    mx = max(mx, (unsigned)__builtin_amdgcn_update_dpp(0, (int)mx, 0x128, 0xf, 0xf, true));
    unsigned ex = max(max(__builtin_amdgcn_readlane(mx, 0), __builtin_amdgcn_readlane(mx, 16)), max(__builtin_amdgcn_readlane(mx, 32), __builtin_amdgcn_readlane(mx, 48))) >> 7;
    ex = min(max(ex, 1u), 253u);
    const float sc = __uint_as_float((254u - ex) << 23), isc = __uint_as_float(ex << 23);
    if (lane == 0) *(LAS f32x2*)(sm + SY + 48 + ch * 8) = (f32x2){2.f * dt * isc, bf2f(k.dsk)};
    const f32x2 c = (f32x2){bf2f(k.c0), bf2f(k.c1)} * sc;
    const f32x2 w1 = cexp_at(a2, rv, 1.f), e256 = cexp_at(a2, rv, 256.f), e256r = crot(e256), e512 = cmul(e256, e256, e256r), e1024 = cmul(e512, e512, crot(e512));
    const f32x2 w1r = crot(w1), w2 = cmul(w1, w1, w1r), w2r = crot(w2), w4 = cmul(w2, w2, w2r), w4r = crot(w4), cr = crot(c);
    f32x2 u = c, u4 = cmul(c, w4, w4r), u2 = cmul(e256, c, cr);
#pragma unroll
    for (int i = 0; i < 4; ++i) {
      put(i, u); put(4 + i, u4); put(8 + i, u2);
      if (i < 3) { u = cmul(u, w1, w1r); u4 = cmul(u4, w1, w1r); u2 = cmul(u2, w2, w2r); }
    }
    const f32x2 u512 = cmul(e512, c, cr), u768 = cmul(u512, e256, e256r), u1024 = cmul(e1024, c, cr), u1024r = crot(u1024);
    const f32x2 u1536 = cmul(e512, u1024, u1024r);
    put(12, u512); put(13, cmul(u512, w4, w4r)); put(14, u768); put(15, cmul(u768, w4, w4r));
    put(16, u1024); put(17, cmul(u1024, e256, e256r)); put(18, u1536); put(19, cmul(u1536, e256, e256r)); put(20, cmul(e1024, u1024, u1024r));
  }
  asm volatile("" ::: "memory");
  STAMP(p, 17);
  {
    const int h = lane >> 5, b = lane & 31;
    const LAS char* ua = sm + KG_TAB + w * KG_WAVE + b * KG_ROW + h * 16;
    f32x16 acc = {};
    f16x8 fa[8], fb[8];
#pragma unroll
    for (int s = 0; s < 8; ++s) { fa[s] = *(const LAS f16x8*)(ua + s * 32); fb[s] = *(const LAS f16x8*)(ua + 21 * KG_ROW + s * 32); }
    __builtin_amdgcn_sched_barrier(0);
#pragma unroll
    for (int s = 0; s < 8; ++s) acc = __builtin_amdgcn_mfma_f32_32x32x16_f16(fa[s], fb[s], acc, 0, 0, 0);
    STAMP(p, 18);
    LAS _Float16* kt = (LAS _Float16*)(sm + KG_K + ch * KG_KCH) + 8 + (w & 1);
    auto add = [&](int i, float v) { kt[2 * i] = (_Float16)v; };
    const int i0 = 4 * h + 8 * b, i1 = h ? 384 + 2 * b : 256 + 4 * b, i1s = h ? 64 : 2;
#pragma unroll
    for (int r = 0; r < 4; ++r) add(i0 + r, acc[r]);
    add(i1, acc[4]); add(i1 + 1, acc[5]); add(i1 + i1s, acc[6]); add(i1 + i1s + 1, acc[7]);
    add(512 + 128 * h + b, acc[8]);
    if (h == 0) { add(544 + b, acc[9]); add(576 + b, acc[10]); add(608 + b, acc[11]); }
  }
}
DEV void kexpand(LAS char* sm, int hw, int lane) {
  const int ch = hw >> 1;
  const LAS unsigned* kt = (const LAS unsigned*)(sm + KG_K + ch * KG_KCH) + 4;
  LAS char* kc = sm + KG_TAB + ch * 2 * KG_WAVE;
  auto sum2 = [&](unsigned hi, unsigned lo) {
    const unsigned a = __builtin_amdgcn_perm(hi, lo, 0x05040100u), b = __builtin_amdgcn_perm(hi, lo, 0x07060302u);
    return __builtin_bit_cast(unsigned, __builtin_bit_cast(f16x2, a) + __builtin_bit_cast(f16x2, b));
  };
  auto emit = [&](int pp, const unsigned (&pr)[10]) {
#pragma unroll
    for (int sg = 0; sg < 4; ++sg) *(LAS v4u*)(kc + sg * CV_KS + (516 - 2 * pp) * 8) = (v4u){pr[6 + sg], pr[4 + sg], pr[2 + sg], pr[sg]};
  };
  {
    const int pp = (hw & 1) * 64 + lane;
    unsigned kv[12], pr[10], ev[6];
#pragma unroll
    for (int i = 0; i < 11; ++i) {
      const int lag = 8 * pp - 3 + i;
      const int code = (int)(pk_bf16((float)lag, 0.f) & 0xffffu) - 0x4280;
      kv[i] = kt[lag < 128 ? lag : code];
    }
    kv[11] = kv[10];
#pragma unroll
    for (int i = 0; i < 6; ++i) ev[i] = sum2(kv[2 * i], kv[2 * i + 1]);
#pragma unroll
    for (int i = 0; i < 5; ++i) { pr[2 * i] = ev[i]; pr[2 * i + 1] = __builtin_amdgcn_alignbit(ev[i], ev[i + 1], 16); }
    emit(pp, pr);
  }
  {
    const int pp = 128 + (hw & 1) * 64 + lane;
    const unsigned ab = sum2(kt[384 + pp], kt[385 + pp]);
    const unsigned a0 = sum2(kt[pp == 128 ? 511 : 384 + pp], kt[384 + pp]);
    const unsigned aa = __builtin_amdgcn_perm(ab, ab, 0x03020302u), bb = __builtin_amdgcn_perm(ab, ab, 0x01000100u);
    const unsigned pr[10] = {a0, aa, aa, aa, aa, aa, (pp & 1) ? ab : aa, (pp & 1) ? bb : ab, bb, bb};
    emit(pp, pr);
  }
  if (lane < 32) {
    const int cpy = (hw & 1) * 2 + (lane >> 4), i = lane & 15;
    *(LAS v2u*)(kc + cpy * CV_KS + (i < 6 ? i : 512 + i) * 8) = (v2u){0u, 0u};
  }
}

DEV void g1_loader(const Params& p, Ts& ts, LAS char* sm, int rt, int ct, int rot, int hw, int lane) {
  const char* gb = hw ? (const char*)p.w1 + ct * 256 : (const char*)p.x + (size_t)rt * 128 * 1024;
  const int cstep = hw ? 64 * F * 2 : 128;
  unsigned v[4];
#pragma unroll
  for (int i = 0; i < 4; ++i) {
    const int rx = i * 8 + (lane >> 3), rw = i * 4 + (lane >> 4);
    v[i] = hw ? rw * (F * 2) + (((lane & 15) ^ ((rw & 3) << 2) ^ (i & 2)) << 4) : rx * 1024 + (((lane & 7) ^ ((rx >> 1) & 7)) << 4);
  }
  const int gstep = hw ? 16 * F * 2 : 32768;
  __builtin_amdgcn_s_setprio(LOADER_PRIO);
  __builtin_amdgcn_s_sleep(LOADER_DELAY);
  unsigned slot = (unsigned)(size_t)sm + hw * 16384;
#pragma unroll 1
  for (int c = 0; c < 8; ++c) {
    const char* g0 = gb + (size_t)(((c + rot) & 7) * cstep);
    const char *g1 = g0 + gstep, *g2 = g1 + gstep, *g3 = g2 + gstep;
#define GL4(S) "global_load_lds_dwordx4 %0, " S "\n\ts_add_u32 m0, m0, 0x400\n\ts_nop 0\n\tglobal_load_lds_dwordx4 %1, " S "\n\ts_add_u32 m0, m0, 0x400\n\ts_nop 0\n\t" \
               "global_load_lds_dwordx4 %2, " S "\n\ts_add_u32 m0, m0, 0x400\n\ts_nop 0\n\tglobal_load_lds_dwordx4 %3, " S "\n\ts_add_u32 m0, m0, 0x400\n\ts_nop 0\n\t"
    asm volatile("s_mov_b32 m0, %8\n\ts_nop 0\n\t" GL4("%4") GL4("%5") GL4("%6") GL4("%7")
                 :: "v"(v[0]), "v"(v[1]), "v"(v[2]), "v"(v[3]), "s"(g0), "s"(g1), "s"(g2), "s"(g3), "s"(slot) : "memory", "scc");
#undef GL4
    slot = slot + G1_SLOT >= (unsigned)(size_t)sm + G1_R * G1_SLOT ? slot - (G1_R - 1) * G1_SLOT : slot + G1_SLOT;
    if (c == 0) STAMP(p, 1);
    if (c == 1) STAMP(p, 2);
    if (c > 0) {
      asm volatile("s_waitcnt vmcnt(16)" ::: "memory");
      if (c == 1) STAMP(p, 3);
      if (c == 2) STAMP(p, 4);
      __builtin_amdgcn_s_barrier();
    }
  }
  drain();
  __builtin_amdgcn_s_barrier();
  __builtin_amdgcn_s_setprio(0);
}
struct G1Frag { v4u a[4], b[4]; };
DEV void g1_main(const Params& p, Ts& ts, LAS char* sm, rsrc_t rs, int rt, int ct, int w, int lane, v2u tag) {
  const int wf = w & 1, wt = w >> 1, g = lane >> 4, r16 = lane & 15;
  f32x4 acc[4][4] = {};
  const int q4 = r16 >> 2, p4 = lane & 3;
  int aoff[2], boff[2];
#pragma unroll
  for (int i = 0; i < 2; ++i) aoff[i] = 16384 + (8 * g + q4) * 256 + (((8 * wf + 4 * i + (p4 >> 1)) ^ (q4 << 2) ^ ((g & 1) << 1)) << 4) + 8 * (p4 & 1);
  const int tok0 = wt * 64 + r16;
#pragma unroll
  for (int s = 0; s < 2; ++s) boff[s] = tok0 * 128 + (((4 * s + g) ^ ((tok0 >> 1) & 7)) << 4);
  auto rd = [&](int c, int s) {
    G1Frag f;
    LAS char* base = sm + (c % G1_R) * G1_SLOT;
#pragma unroll
    for (int i = 0; i < 4; ++i) {
      const int o = (aoff[i >> 1] ^ ((i & 1) << 5)) + s * 8192;
      const v2u a0 = __builtin_bit_cast(v2u, __builtin_amdgcn_ds_read_tr16_b64_v4i16((LAS s16x4*)(base + o)));
      const v2u a1 = __builtin_bit_cast(v2u, __builtin_amdgcn_ds_read_tr16_b64_v4i16((LAS s16x4*)(base + o + 1024)));
      f.a[i] = (v4u){a0.x, a0.y, a1.x, a1.y};
      f.b[i] = *(const LAS v4u*)(base + boff[s] + 2048 * i);
    }
    return f;
  };
  G1Frag fr[16];
#pragma unroll
  for (int st = -1; st < 16; ++st) {
    const int ld = st + 1;
    if (ld < 16) {
      if ((ld & 1) == 0) lds_barrier();
      if (ld == 0) STAMP(p, 19);
      if (ld == 2) STAMP(p, 20);
      if (ld == 4) STAMP(p, 24);
      if (ld == 8) STAMP(p, 25);
      if (ld == 12) STAMP(p, 26);
      if (ld == 14) STAMP(p, 29);
      fr[ld] = rd(ld >> 1, ld & 1);
    }
    if (st >= 0) {
#pragma unroll
      for (int i = 0; i < 4; ++i)
#pragma unroll
        for (int j = 0; j < 4; ++j)
          acc[i][j] = __builtin_amdgcn_mfma_f32_16x16x32_bf16(__builtin_bit_cast(bf16x8, fr[st].a[i]), __builtin_bit_cast(bf16x8, fr[st].b[j]), acc[i][j], 0, 0, 0);
#pragma unroll
      for (int i = 0; i < 12; ++i) {
        __builtin_amdgcn_sched_group_barrier(0x8, 1, 0);
        __builtin_amdgcn_sched_group_barrier(0x100, 1, 0);
      }
      __builtin_amdgcn_sched_group_barrier(0x8, 4, 0);
    }
    __builtin_amdgcn_sched_barrier(0);
  }
  STAMP(p, 3);
  __builtin_amdgcn_s_setprio(2);
  lds_wait(sm, 8);
  unsigned gs[4], gc[4];
#pragma unroll
  for (int i = 0; i < 4; ++i) {
    gs[i] = *(const LAS unsigned*)(sm + GR + (tok0 + 16 * i) * 4);
    gc[i] = *(const LAS unsigned*)(sm + GR + 512 + (wf * 64 + r16 + 16 * i) * 4);
  }
  STAMP(p, 30);
  float c1[4], sd[4];
#pragma unroll
  for (int j = 0; j < 4; ++j) {
    const unsigned m2 = __builtin_amdgcn_perm(gs[j], gs[j], 0x01000100u) ^ 0x80008000u;
    const v2u bm = {g == 0 ? m2 : 0u, 0u};
#pragma unroll
    for (int i = 0; i < 4; ++i)
      acc[i][j] = __builtin_amdgcn_mfma_f32_16x16x16bf16_1k(__builtin_bit_cast(s16x4, (v2u){gc[i], 0u}), __builtin_bit_cast(s16x4, bm), acc[i][j], 0, 0, 0);
    sd[j] = bfhi(gs[j]);
    c1[j] = __builtin_amdgcn_rcpf(sd[j]) * -1.44269504088896f;
  }
  const int hbase = WS_HF + ((2 * rt + wt) * 56 + 4 * ct + 2 * wf) * 4096 + lane * 16;
#pragma unroll
  for (int m = 0; m < 2; ++m)
#pragma unroll
    for (int j = 0; j < 4; ++j) {
      v4u o;
#pragma unroll
      for (int e = 0; e < 4; ++e) {
        const f32x4 a4 = acc[2 * m + (e >> 1)][j];
        const f32x2 t = (e & 1) ? a4.zw : a4.xy;
        const f32x2 u = t * c1[j];
        const f32x2 ex = {__builtin_amdgcn_exp2f(u.x), __builtin_amdgcn_exp2f(u.y)};
        const f32x2 dn = __builtin_elementwise_fma(ex, (f32x2){sd[j], sd[j]}, (f32x2){sd[j], sd[j]});
        const f32x2 r = t * (f32x2){__builtin_amdgcn_rcpf(dn.x), __builtin_amdgcn_rcpf(dn.y)};
        o[e] = pk_bf16(r.x, r.y);
      }
      __builtin_amdgcn_raw_buffer_store_b128(o, rs, hbase + m * 4096 + j * 1024, 0, SC1);
      __builtin_amdgcn_sched_barrier(0);
    }
  STAMP(p, 4);
  publish(rs, sm, 1, 4, WS_FH + (rt * 16 + ct) * FSTR, tag, lane);
  __builtin_amdgcn_s_setprio(0);
}

DEV int ks_of(int w, int rl, int i) { return 7 * ((w + 2 * rl) & 7) + i; }
template <int LO, int HI> DEV void w2_load(rsrc_t rs, int woff, int w, int rl, v4u (&wr)[28]) {
#pragma unroll
  for (int i = LO; i < HI; ++i) wr[i] = __builtin_amdgcn_raw_buffer_load_b128(rs, woff + (i & 3) * 1024, ks_of(w, rl, i >> 2) * 4096, SC1);
}

DEV void conv(const Params& p, Ts& ts, LAS char* sm, rsrc_t rs, int b, int hw, int lane, v2u tag, int woff, int w, int rl, v4u (&wr)[28]) {
  const int ch = hw >> 1, eh = hw & 1, g = lane >> 4, r16 = lane & 15, d = chan_of(b, ch);
  LAS char* xs = sm + CV_X + ch * CV_XS;
  if (eh == 0 && lane < 34) *(LAS v4u*)(xs + (lane < 32 ? lane : 256 + lane) * 16) = (v4u){0u, 0u, 0u, 0u};
  lds_wait(sm, 3);
  STAMP(p, 14);
  v4u xv[2];
#pragma unroll
  for (int i = 0; i < 2; ++i)
    xv[i] = __builtin_amdgcn_raw_buffer_load_b128(rs, WS_XT + (hw * 64 + i * 32 + (lane >> 1)) * 8192 + (b & 7) * 1024 + (2 * (b >> 3) + (lane & 1)) * 16, 0, SC1);
  if (hw < 2) w2_load<0, W2_EARLY>(rs, woff, w, rl, wr);
  w2_load<W2_EARLY, 28>(rs, woff, w, rl, wr);
  __builtin_amdgcn_sched_barrier(0);
  STAMP(p, 15);
#pragma unroll
  for (int i = 0; i < 2; ++i) *(LAS v4u*)(sm + CV_X + (lane & 1) * CV_XS + 512 + (hw * 64 + i * 32 + (lane >> 1)) * 16) = xv[i];
  kexpand(sm, hw, lane);
  wave_sync(sm, 4, 4);
  STAMP(p, 6);
  const LAS char* ka = sm + KG_TAB + ch * 2 * KG_WAVE + (r16 & 3) * CV_KS + (517 - (r16 >> 2) + 2 * g) * 8;
  const LAS char* xa = xs + 512 + r16 * 32 + g * 16;
  auto lda = [&](const LAS char* ptr) {
    const v2u lo = *(const volatile LAS v2u*)ptr, hi = *(const volatile LAS v2u*)(ptr + 8);
    return __builtin_bit_cast(f16x8, (v4u){lo.x, lo.y, hi.x, hi.y});
  };
  f32x4 acc[8];
#pragma unroll
  for (int a = 0; a < 8; ++a) acc[a] = (f32x4){0.f, 0.f, 0.f, 0.f};
  const LAS char* ke = ka - 256 * eh;
  const LAS char* xe = xa - 256 * eh;
  static_for<0, 4>([&](auto ee) {
    constexpr int e = decltype(ee)::v;
    f16x8 A[8], B[8];
#pragma unroll
    for (int i = 0; i < 8; ++i) { A[i] = lda(ke - 64 * (e + 1) - 512 * i); B[i] = *(const LAS f16x8*)(xe - 64 * (e + 1) + 512 * i); }
#pragma unroll
    for (int a = 0; a < 8; ++a)
#pragma unroll
      for (int dd = 0; dd <= a; ++dd) acc[a] = __builtin_amdgcn_mfma_f32_16x16x32_f16(A[dd], B[a - dd], acc[a], 0, 0, 0);
    __builtin_amdgcn_sched_barrier(0);
  });
  LAS f32x4* ex = (LAS f32x4*)(sm + CV_E);
  f32x4 own[4];
#pragma unroll
  for (int i = 0; i < 4; ++i) {
    ex[(hw * 4 + i) * 64 + lane] = eh ? acc[i] : acc[4 + i];
    own[i] = eh ? acc[4 + i] : acc[i];
  }
  {
    const f16x8 A0 = lda(ka);
#pragma unroll
    for (int i = 0; i < 4; ++i) own[i] = __builtin_amdgcn_mfma_f32_16x16x32_f16(A0, *(const LAS f16x8*)(xa + 512 * (4 * eh + i)), own[i], 0, 0, 0);
  }
  STAMP(p, 7);
  wave_sync(sm, 6 + ch, 2);
  const f32x2 md = *(const LAS f32x2*)(sm + SY + 48 + ch * 8);
  const float mult = md.x, dsk = md.y;
#pragma unroll
  for (int i = 0; i < 4; ++i) {
    const f32x4 y = (own[i] + ex[((hw ^ 1) * 4 + i) * 64 + lane]) * mult;
    const int t0 = 256 * (4 * eh + i) + 16 * r16 + 4 * g;
    const v2u xr = *(const LAS v2u*)(xs + 512 + t0 * 2);
    const unsigned xr0 = xr.x, xr1 = xr.y;
    const f16x2 x0 = __builtin_bit_cast(f16x2, xr0), x1 = __builtin_bit_cast(f16x2, xr1);
    const unsigned y01 = pk_bf16(y[0], y[1]), y23 = pk_bf16(y[2], y[3]);
    const unsigned d01 = pk_bf16((float)x0.x * dsk, (float)x0.y * dsk), d23 = pk_bf16((float)x1.x * dsk, (float)x1.y * dsk);
    const v2u o = {pk_bf16(bflo(y01) + bflo(d01), bfhi(y01) + bfhi(d01)), pk_bf16(bflo(y23) + bflo(d23), bfhi(y23) + bfhi(d23))};
    __builtin_amdgcn_raw_buffer_store_b64(o, rs, WS_ST + d * 4096 + t0 * 2, 0, SC1);
  }
  publish(rs, sm, 2, 4, WS_FS + b * FSTR, tag, lane);
}

DEV void gemm2(const Params& p, Ts& ts, LAS char* sm, rsrc_t rs, int rt2, int ct2, int rl, bool isL, int w, int lane, v2u tag, v4u (&wr)[28]) {
  const int g = lane >> 4, hb = (lane >> 3) & 1, cb = lane & 7;
  if (w == 0) { wait_flags(rs, WS_FH + (rt2 >> 1) * 16 * FSTR, 14, tag, lane); lds_set(sm, 5); }
  else lds_wait(sm, 5);
  STAMP(p, 10);
  v4u ha[28];
  const int hoff = WS_HF + rt2 * 56 * 4096 + lane * 16;
  auto hload = [&](int i) { return __builtin_amdgcn_raw_buffer_load_b128(rs, hoff + (i & 3) * 1024, ks_of(w, rl, i >> 2) * 4096, SC1); };
#pragma unroll
  for (int i = 0; i < 16; ++i) ha[i] = hload(i);
  __builtin_amdgcn_sched_barrier(0);
  f32x4 acc[4][4] = {};
  auto mm = [&](int ks, int mt) {
#pragma unroll
    for (int t = 0; t < 4; ++t)
      acc[mt][t] = __builtin_amdgcn_mfma_f32_16x16x32_bf16(__builtin_bit_cast(bf16x8, ha[4 * ks + mt]), __builtin_bit_cast(bf16x8, wr[4 * ks + t]), acc[mt][t], 0, 0, 0);
  };
#pragma unroll
  for (int ks = 0; ks < 4; ++ks) {
#pragma unroll
    for (int mt = 0; mt < 4; ++mt) mm(ks, mt);
    __builtin_amdgcn_sched_barrier(0);
    if (ks < 2) {
#pragma unroll
      for (int i = 0; i < 6; ++i) { const int e = 6 * ks + i; ha[16 + 4 * (e % 3) + e / 3] = hload(16 + 4 * (e % 3) + e / 3); }
    }
    __builtin_amdgcn_sched_barrier(0);
  }
  lds_wait(sm, w < 4 ? 1 : 2, w < 4 && isL ? 0u : 4u);
  LAS f32x4* red = (LAS f32x4*)(sm + (w < 4 ? 32768 + w * 16384 : (w < 6 ? (w - 4) * 16384 : 98304 + (w - 6) * 16384)));
#pragma unroll
  for (int mt = 0; mt < 4; ++mt) {
#pragma unroll
    for (int ks = 4; ks < 7; ++ks) mm(ks, mt);
    if (mt > 0) {
#pragma unroll
      for (int t = 0; t < 4; ++t) red[((mt - 1) * 4 + t) * 64 + lane] = acc[mt - 1][t];
    }
#pragma unroll
    for (int i = 0; i < 4; ++i) { __builtin_amdgcn_sched_group_barrier(0x8, 3, 0); __builtin_amdgcn_sched_group_barrier(0x200, 1, 0); }
    __builtin_amdgcn_sched_barrier(0);
  }
#pragma unroll
  for (int t = 0; t < 4; ++t) red[(12 + t) * 64 + lane] = acc[3][t];
  STAMP(p, 11);
  const int tok = rt2 * 64 + 16 * w + 4 * g, chn = ct2 * 64 + 8 * cb + 4 * hb;
  v2u xv[4];
  f32x4 o[4];
  if (w < 4) {
#pragma unroll
    for (int i = 0; i < 4; ++i) xv[i] = *(const GAS v2u*)((const GAS char*)p.x + (size_t)(tok + i) * 1024 + chn * 2);
    wave_sync(sm, 10, 4);
#pragma unroll
    for (int t = 0; t < 4; ++t) {
      const LAS f32x4* r0 = (const LAS f32x4*)(sm + 32768) + (w * 4 + t) * 64 + lane;
      o[t] = (r0[0] + r0[1024]) + (r0[2048] + r0[3072]);
    }
    if (w == 0) {
      lds_wait(sm, 2, 4u);
      __builtin_amdgcn_s_sleep(16);
      wait_flags(rs, WS_FS + ct2 * 32 * FSTR, 32, tag, lane);
      lds_set(sm, 9);
    } else lds_wait(sm, 9);
#pragma unroll
    for (int i = 0; i < 2; ++i) {
      const int c = 16 * w + 8 * i + (lane >> 3);
      *(LAS v4u*)(sm + G2_S + c * 144 + (lane & 7) * 16) =
          __builtin_amdgcn_raw_buffer_load_b128(rs, WS_ST + (ct2 * 64 + c) * 4096 + rt2 * 128 + (lane & 7) * 16, 0, SC1);
    }
  }
  STAMP(p, 28);
  lds_barrier();
  STAMP(p, 12);
  if (w < 4) {
#pragma unroll
    for (int t = 0; t < 4; ++t) {
      const LAS f32x4* r0 = (const LAS f32x4*)sm + (w * 4 + t) * 64 + lane;
      o[t] += (r0[0] + r0[1024]) + (r0[6144] + r0[7168]);
    }
    v2u sv[4];
#pragma unroll
    for (int u = 0; u < 4; ++u) sv[u] = *(const LAS v2u*)(sm + G2_S + (8 * cb + 4 * hb + u) * 144 + 32 * w + 8 * g);
#pragma unroll
    for (int i = 0; i < 4; ++i) {
      v2u res;
#pragma unroll
      for (int u = 0; u < 2; ++u) {
        const unsigned ob = pk_bf16(o[2 * u][i], o[2 * u + 1][i]);
        const unsigned gb = pk_bf16(silu_f(bflo(ob)), silu_f(bfhi(ob)));
        const unsigned s0 = sv[2 * u][i >> 1], s1 = sv[2 * u + 1][i >> 1];
        const float sa = (i & 1) ? bfhi(s0) : bflo(s0), sb = (i & 1) ? bfhi(s1) : bflo(s1);
        const unsigned pr = pk_bf16(sa * bflo(gb), sb * bfhi(gb));
        const unsigned xx = xv[i][u];
        res[u] = pk_bf16(bflo(xx) + bflo(pr), bfhi(xx) + bfhi(pr));
      }
      *(GAS v2u*)((GAS char*)p.out + (size_t)(tok + i) * 1024 + chn * 2) = res;
    }
  }
  STAMP(p, 13);
}

__global__ void __launch_bounds__(512, 2) fused_kernel(Params p) {
  __shared__ __attribute__((aligned(16))) char smem[LDS_BYTES];
  LAS char* sm = (LAS char*)smem;
  const int tid = threadIdx.x, lane = tid & 63, w = __builtin_amdgcn_readfirstlane(tid >> 6);
  const int b = blockIdx.x, xcd = b & 7, idx = b >> 3;
  const bool isL = idx < 4;
  const int ii = idx - 4, rt = (xcd & 3) * 4 + (ii & 3), ct = (xcd >> 2) * 7 + (ii >> 2);
  const int rl = idx >> 3, rt2 = (xcd & 3) * 8 + (xcd >> 2) * 4 + rl, ct2 = idx & 7;
  const int t8 = (xcd & 3) * 64 + (xcd >> 2) * 32 + idx;
  Ts ts = {};
  STAMP(p, 0);
  const rsrc_t rs = __builtin_amdgcn_make_buffer_rsrc(p.ws, 0, WS_BYTES, 0x00020000);
  asm volatile("" ::"s"(p.x), "s"(p.lar), "s"(p.aim), "s"(p.cre), "s"(p.cim), "s"(p.ldt), "s"(p.dsk), "s"(p.w1), "s"(p.w2), "s"(p.ws));
  v2u ep;
  KPar kp;
  v4u early[8];
  unsigned pf;
  asm volatile("global_load_dwordx2 %0, %1, %2 sc1" : "=v"(ep) : "v"(WS_EPOCH), "s"(p.ws));
  if (w < 4) {
    const unsigned d2 = chan_of(b, w >> 1) * 2, o = d2 * NM + ((w & 1) * 64 + lane) * 2, o2 = o ^ 128;
    asm volatile("global_load_ushort %0, %8, %11\n\tglobal_load_ushort %1, %9, %12\n\tglobal_load_ushort %2, %9, %13\n\t"
                 "global_load_ushort %3, %9, %14\n\tglobal_load_ushort %4, %9, %15\n\tglobal_load_ushort %5, %10, %14\n\t"
                 "global_load_ushort %6, %10, %15\n\tglobal_load_ushort %7, %8, %16"
                 : "=&v"(kp.ldt), "=&v"(kp.lar), "=&v"(kp.aim), "=&v"(kp.c0), "=&v"(kp.c1), "=&v"(kp.c2), "=&v"(kp.c3), "=&v"(kp.dsk)
                 : "v"(d2), "v"(o), "v"(o2), "s"(p.ldt), "s"(p.lar), "s"(p.aim), "s"(p.cre), "s"(p.cim), "s"(p.dsk));
  }
  if (w >= 6) {
    const int piece = isL ? 0 : (b - 32) * 2;
    const char* sb = w == 6 ? (const char*)p.x + (size_t)t8 * 8192 : (const char*)p.w2 + (size_t)(piece % 56) * 32768 + (piece / 56) * 128;
    const unsigned o = w == 6 ? lane * 16 : (16 * ((lane >> 3) & 1) + 4 * (lane >> 4)) * 1024 + (lane & 7) * 16;
    const unsigned o2 = o + (w == 6 ? 4096 : 32768);
    asm volatile("global_load_dwordx4 %0, %8, %10\n\tglobal_load_dwordx4 %1, %8, %10 offset:1024\n\tglobal_load_dwordx4 %2, %8, %10 offset:2048\n\t"
                 "global_load_dwordx4 %3, %8, %10 offset:3072\n\tglobal_load_dwordx4 %4, %9, %10\n\tglobal_load_dwordx4 %5, %9, %10 offset:1024\n\t"
                 "global_load_dwordx4 %6, %9, %10 offset:2048\n\tglobal_load_dwordx4 %7, %9, %10 offset:3072"
                 : "=&v"(early[0]), "=&v"(early[1]), "=&v"(early[2]), "=&v"(early[3]), "=&v"(early[4]), "=&v"(early[5]), "=&v"(early[6]), "=&v"(early[7])
                 : "v"(o), "v"(o2), "s"(sb));
  }
  if ((w & 6) == 4) {
    const u64 pc = __builtin_amdgcn_s_getpc() & ~(u64)255;
    asm volatile("global_load_dword %0, %1, %2" : "=v"(pf) : "v"(min(idx * 1024 + (w - 4) * 512 + (lane & 3) * 128, CODE_BYTES - 128)), "s"(pc));
    if (isL) {
      drain();
#pragma unroll 1
      for (int i = 0; i < 8; ++i) bar();
    } else g1_loader(p, ts, sm, rt, ct, 2 * (ii & 3) + 2 * (ii >> 2), w - 4, lane);
    asm volatile("" ::"v"(pf));
  }
  __builtin_amdgcn_sched_barrier(0);
  const int woff = WS_W2 + ct2 * 56 * 4096 + lane * 16;
  v4u wr[28];
  auto make_tag = [&]() {
    unsigned lo, hi;
    asm volatile("v_readfirstlane_b32 %0, %2\n\tv_readfirstlane_b32 %1, %3" : "=s"(lo), "=s"(hi) : "v"(ep.x), "v"(ep.y));
    const u64 t = (((u64)hi << 32) | lo) + 1ull;
    return (v2u){(unsigned)t, (unsigned)(t >> 32)};
  };
  v2u tag;
  if (w == 4 || w == 5) {
    tag = make_tag();
  } else if (w < 4) {
    v4u cw[16];
    const int cg = (xcd & 3) * 4 + idx;
    if (isL) cs_load(p, (xcd >> 2) * 14 + min(cg, 13), w, lane, cw);
    if (w == 0 && lane < 12) *(LAS unsigned*)(sm + SY + lane * 4) = 0u;
    if (w == 1 && lane < 8) *(LAS unsigned*)(sm + KG_K + (lane >> 2) * KG_KCH + (lane & 3) * 4) = 0u;
    __builtin_amdgcn_sched_barrier(0);
    drain();
    __builtin_amdgcn_sched_barrier(0);
    asm volatile("" : "+v"(kp.ldt), "+v"(kp.lar), "+v"(kp.aim), "+v"(kp.c0), "+v"(kp.c1), "+v"(kp.c2), "+v"(kp.c3), "+v"(kp.dsk));
    tag = make_tag();
    STAMP(p, 1);
    if (isL) cs_role(sm, rs, (xcd >> 2) * 14 + cg, cg < 14, w, lane, tag, cw);
    STAMP(p, 16);
    kgen(p, ts, sm, kp, w, lane);
    STAMP(p, 2);
    if (isL) {
      asm volatile("s_waitcnt lgkmcnt(0)" ::: "memory");
#pragma unroll 1
      for (int i = 0; i < 7; ++i) bar();
    } else g1_main(p, ts, sm, rs, rt, ct, w, lane, tag);
    STAMP(p, 5);
    w2_load<0, 28>(rs, woff, w, rl, wr);
    __builtin_amdgcn_sched_barrier(0);
    STAMP(p, 23);
  } else {
    drain();
    __builtin_amdgcn_sched_barrier(0);
    asm volatile("" : "+v"(early[0]), "+v"(early[1]), "+v"(early[2]), "+v"(early[3]), "+v"(early[4]), "+v"(early[5]), "+v"(early[6]), "+v"(early[7]));
    tag = make_tag();
    STAMP(p, 1);
    if (w == 6) ln_wave(p, ts, rs, t8, lane, tag, early);
    else if (!isL) { w2_piece_store(rs, (b - 32) * 2, lane, early); w2_piece_store(rs, (b - 32) * 2 + 1, lane, early + 4); }
    STAMP(p, 27);
    bar();
    if (!isL) bar();
    drain();
    if (lane == 0 && (w == 6 || !isL)) __builtin_amdgcn_raw_buffer_store_b64(tag, rs, w == 6 ? WS_FX + b * FSTR : WS_FW + (b - 32) * FSTR, 0, SC1);
    STAMP(p, 22);
    if (isL) bar();
    bar(); bar(); bar();
    if (w == 7) {
      const int l28 = lane < 28 ? lane : (isL ? min(lane, 55) + 84 : 27);
      bool first = true;
#pragma unroll 1
      for (unsigned spins = 0; spins < SPIN_CAP; ++spins) {
        asm volatile("" ::: "memory");
        const v2u f = __builtin_amdgcn_raw_buffer_load_b64(rs, WS_FW + (ct2 * 28 + l28) * FSTR, 0, SC1);
        __builtin_amdgcn_sched_barrier(0);
        if (first) { bar(); first = false; }
        if (__all(f.x == tag.x && f.y == tag.y)) break;
        __builtin_amdgcn_s_sleep(8);
      }
    } else bar();
    bar();
    const int poff = w == 7 ? WS_FX + lane * FSTR : WS_GS + (isL ? 0 : rt * 1024) + lane * 8;
    const int pst1 = w == 7 ? 64 * FSTR : 512, pst2 = w == 7 ? 128 * FSTR : WS_GC - WS_GS + (isL ? 0 : (ct - rt) * 1024);
    const unsigned tx = w == 7 ? tag.y : tag.x, mx = w == 7 ? ~0u : 0u;
    v2u f[4];
#pragma unroll
    for (int i = 0; i < 4; ++i) f[i] = __builtin_amdgcn_raw_buffer_load_b64(rs, poff, (i & 1) * pst1 + (i >> 1) * pst2, SC1);
    w2_load<0, W2_EARLY>(rs, woff, w, rl, wr);
    __builtin_amdgcn_sched_barrier(0);
    bar();
#pragma unroll 1
    for (unsigned spins = 0; spins < SPIN_CAP; ++spins) {
      unsigned bad = 0;
#pragma unroll
      for (int i = 0; i < 4; ++i) bad |= (f[i].y ^ tx) | ((f[i].x ^ tag.x) & mx);
      if (__all(bad == 0u)) break;
      __builtin_amdgcn_s_sleep(8);
      asm volatile("" ::: "memory");
#pragma unroll
      for (int i = 0; i < 4; ++i) f[i] = __builtin_amdgcn_raw_buffer_load_b64(rs, poff, (i & 1) * pst1 + (i >> 1) * pst2, SC1);
    }
    if (w == 6) {
#pragma unroll
      for (int i = 0; i < 4; ++i) *(LAS unsigned*)(sm + GR + i * 256 + lane * 4) = f[i].x;
      asm volatile("" ::: "memory");
    }
    lds_set(sm, w == 6 ? 8 : 3);
    if (b == 0 && w == 7 && lane == 0) __builtin_amdgcn_raw_buffer_store_b64(tag, rs, WS_EPOCH, 0, SC1);
  }
  if (w >= 4) {
    STAMP(p, 9);
    conv(p, ts, sm, rs, b, w - 4, lane, tag, woff, w, rl, wr);
    STAMP(p, 8);
  }
  gemm2(p, ts, sm, rs, rt2, ct2, rl, isL, w, lane, tag, wr);
  {
    GAS u64* dbg = (GAS u64*)(p.ws + (32 << 20));
    if (w == 0) while ((unsigned)__builtin_amdgcn_s_memrealtime() - ts.t[0] < 4000u) __builtin_amdgcn_s_sleep(2);
  }
}

__global__ void tiny_first(char* ws) { if (threadIdx.x == 0) ((GAS u64*)(ws + (32 << 20)))[0] = (unsigned)__builtin_amdgcn_s_memrealtime(); }
__global__ void tiny_last(char* ws) { const unsigned z = (unsigned)((GAS u64*)(ws + (32 << 20)))[0]; while ((unsigned)__builtin_amdgcn_s_memrealtime() - z < 6000u) __builtin_amdgcn_s_sleep(2); }
extern "C" void kernel_launch(void* const* d_in, const int* in_sizes, int n_in, void* d_out, int out_size, void* d_ws, size_t ws_size,
                              hipStream_t stream) {
  Params p;
  p.x = (const u16*)d_in[0]; p.lar = (const u16*)d_in[1]; p.aim = (const u16*)d_in[2]; p.cre = (const u16*)d_in[3];
  p.cim = (const u16*)d_in[4]; p.ldt = (const u16*)d_in[5]; p.dsk = (const u16*)d_in[6]; p.w1 = (const u16*)d_in[8];
  p.w2 = (const u16*)d_in[9]; p.out = (u16*)d_out; p.ws = (char*)d_ws;
  fused_kernel<<<256, 512, 0, stream>>>(p);
}
```

```cpp
#include <hip/hip_runtime.h>
#include <hip/hip_bf16.h>
#include <stdint.h>

typedef unsigned short u16;
typedef unsigned long long u64;
typedef unsigned v4u __attribute__((ext_vector_type(4)));
typedef unsigned v2u __attribute__((ext_vector_type(2)));
typedef float f32x2 __attribute__((ext_vector_type(2)));
typedef float f32x4 __attribute__((ext_vector_type(4)));
typedef float f32x16 __attribute__((ext_vector_type(16)));
typedef short s16x4 __attribute__((ext_vector_type(4)));
typedef __bf16 bf16x2 __attribute__((ext_vector_type(2)));
typedef __bf16 bf16x8 __attribute__((ext_vector_type(8)));
typedef _Float16 f16x2 __attribute__((ext_vector_type(2)));
typedef _Float16 f16x8 __attribute__((ext_vector_type(8)));
typedef __amdgpu_buffer_rsrc_t rsrc_t;
#define GAS __attribute__((address_space(1)))
#define LAS __attribute__((address_space(3)))
#define DEV __device__ __forceinline__

constexpr int L = 2048, D = 512, NM = 128, F = 1792;
constexpr int WS_EPOCH = 0;
constexpr int FSTR = 64;
constexpr int WS_FH = 4096;
constexpr int WS_FX = WS_FH + 256 * FSTR;
constexpr int WS_FS = WS_FX + 256 * FSTR;
constexpr int WS_GS = WS_FS + 256 * FSTR;
constexpr int WS_GC = WS_GS + L * 8;
constexpr int WS_XT = 1 << 17;
constexpr int WS_ST = WS_XT + (2 << 20);
constexpr int WS_HF = WS_ST + (2 << 20);
constexpr int WS_BYTES = WS_HF + L * F * 2;
#define L_PRE 2
#define OUT_AUX 16
#define LOADER_PRIO 0
#define LOADER_DELAY 5
constexpr int SC1 = 16;
#define HROT 4
#define EXP_PRIO 2
#define MAIN_PRE_T 3
#define W2_PRE 14
#define W2_EARLY 12
constexpr int CODE_BYTES = 35 * 1024;
constexpr unsigned SPIN_CAP = 1u << 16;
constexpr int G1_SLOT = 32768, G1_R = 4;
constexpr int KG_ROW = 272;
constexpr int KG_WAVE = 53 * KG_ROW;
constexpr int KG_TAB = 3 * G1_SLOT;
constexpr int KG_K = KG_TAB + 4 * KG_WAVE;
constexpr int KG_KCH = 676 * 4;
constexpr int SY = KG_K + 2 * KG_KCH;
constexpr int GR = SY + 128;
constexpr int LDS_USED = GR + 1024;
constexpr int CV_KS = 4288;
constexpr int CV_K = 0;
constexpr int CV_X = 8 * CV_KS;
constexpr int CV_XS = (256 + 2048 + 16) * 2;
constexpr int CV_E = CV_X + 2 * CV_XS;
constexpr int G2_S = 4 * G1_SLOT;
constexpr int LDS_BYTES = 160 * 1024;
static_assert(WS_GC + F * 8 <= WS_XT && G2_S + 64 * 144 <= KG_K && LDS_USED <= LDS_BYTES && CV_E % 16 == 0 && CV_E + 16384 <= 2 * G1_SLOT, "maps");

DEV float bf2f(unsigned v) { return __uint_as_float(v << 16); }
DEV float bflo(unsigned v) { return __uint_as_float(v << 16); }
DEV float bfhi(unsigned v) { return __uint_as_float(v & 0xffff0000u); }
DEV unsigned pk_bf16(float a, float b) { f32x2 f = {a, b}; return __builtin_bit_cast(unsigned, __builtin_convertvector(f, bf16x2)); }
DEV unsigned pk_f16(float a, float b) { f32x2 f = {a, b}; return __builtin_bit_cast(unsigned, __builtin_convertvector(f, f16x2)); }
DEV float rbf(float a) { return bflo(pk_bf16(a, a) & 0xffffu); }
DEV float dot2bf(unsigned a, unsigned b, float c) {
  return __builtin_amdgcn_fdot2_f32_bf16(__builtin_bit_cast(bf16x2, a), __builtin_bit_cast(bf16x2, b), c, false);
}
DEV float silu_f(float v) { return v * __builtin_amdgcn_rcpf(1.f + __builtin_amdgcn_exp2f(v * -1.44269504088896f)); }
DEV void lds_barrier() { asm volatile("s_waitcnt lgkmcnt(0)" ::: "memory"); __builtin_amdgcn_s_barrier(); asm volatile("" ::: "memory"); }
DEV void drain() { asm volatile("s_waitcnt vmcnt(0)" ::: "memory"); }
template <int CTRL> DEV float dpp_add(float v) {
  return v + __builtin_bit_cast(float, __builtin_amdgcn_update_dpp(0, __builtin_bit_cast(int, v), CTRL, 0xf, 0xf, true));
}
DEV float row16_sum(float v) { v = dpp_add<0xb1>(v); v = dpp_add<0x4e>(v); v = dpp_add<0x124>(v); return dpp_add<0x128>(v); }
DEV float swap32_add(float a, float b) {
  auto r = __builtin_amdgcn_permlane32_swap(__float_as_uint(a), __float_as_uint(b), false, false);
  return __uint_as_float(r[0]) + __uint_as_float(r[1]);
}
DEV float swap16_add(float a, float b) {
  auto r = __builtin_amdgcn_permlane16_swap(__float_as_uint(a), __float_as_uint(b), false, false);
  return __uint_as_float(r[0]) + __uint_as_float(r[1]);
}
DEV float rdlane(float v, int l) { return __uint_as_float(__builtin_amdgcn_readlane(__float_as_uint(v), l)); }
template <int I> struct IC { static constexpr int v = I; };
template <int B, int E, class Fn> DEV void static_for(Fn&& f) { if constexpr (B < E) { f(IC<B>{}); static_for<B + 1, E>(f); } }

struct Ts {};
#define STAMP(p, i)

struct Params {
  const u16 *x, *lar, *aim, *cre, *cim, *ldt, *dsk, *w1, *w2;
  u16* out;
  char* ws;
};

DEV void wait_flags(rsrc_t rs, int off, int n, v2u tag, int lane) {
#pragma unroll 1
  for (unsigned spins = 0; spins < SPIN_CAP; ++spins) {
    asm volatile("" ::: "memory");
    const v2u v = __builtin_amdgcn_raw_buffer_load_b64(rs, off + min(lane, n - 1) * FSTR, 0, SC1);
    if (__all(v.x == tag.x && v.y == tag.y)) break;
    __builtin_amdgcn_s_sleep(4);
  }
  __builtin_amdgcn_fence(__ATOMIC_ACQUIRE, "wavefront");
}
DEV void publish(rsrc_t rs, LAS char* sm, int word, unsigned nwaves, int off, v2u tag, int lane) {
  drain();
  if (lane == 0) {
    const unsigned old = __hip_atomic_fetch_add((LAS unsigned*)(sm + SY) + word, 1u, __ATOMIC_RELAXED, __HIP_MEMORY_SCOPE_WORKGROUP);
    if (old == nwaves - 1) __builtin_amdgcn_raw_buffer_store_b64(tag, rs, off, 0, SC1);
  }
}
DEV void wave_sync(LAS char* sm, int word, unsigned n) {
  asm volatile("s_waitcnt lgkmcnt(0)" ::: "memory");
  volatile LAS unsigned* c = (volatile LAS unsigned*)(sm + SY) + word;
  if ((threadIdx.x & 63) == 0) __hip_atomic_fetch_add((LAS unsigned*)c, 1u, __ATOMIC_RELAXED, __HIP_MEMORY_SCOPE_WORKGROUP);
#pragma unroll 1
  for (unsigned spins = 0; spins < 16 * SPIN_CAP && __builtin_amdgcn_readfirstlane(*c) != n; ++spins) {}
  asm volatile("" ::: "memory");
}

DEV void lds_set(LAS char* sm, int word) { *((volatile LAS unsigned*)(sm + SY) + word) = 1u; }
DEV void lds_wait(LAS char* sm, int word, unsigned want = 1u) {
  volatile LAS unsigned* c = (volatile LAS unsigned*)(sm + SY) + word;
#pragma unroll 1
  for (unsigned spins = 0; spins < 16 * SPIN_CAP && __builtin_amdgcn_readfirstlane(*c) != want; ++spins) __builtin_amdgcn_s_sleep(1);
  __builtin_amdgcn_fence(__ATOMIC_ACQUIRE, "wavefront");
  asm volatile("" ::: "memory");
}
DEV void bar() { __builtin_amdgcn_sched_barrier(0); asm volatile("" ::: "memory"); __builtin_amdgcn_s_barrier(); asm volatile("" ::: "memory"); __builtin_amdgcn_sched_barrier(0); }

DEV void ln_wave(const Params& p, Ts& ts, rsrc_t rs, int t8, int lane, v2u tag, bool late, const v4u (&xr)[8]) {
  float s[8], q[8], t[4], rz[2], mz[2];
#pragma unroll
  for (int r = 0; r < 8; ++r) {
    s[r] = 0.f; q[r] = 0.f;
#pragma unroll
    for (int j = 0; j < 4; ++j) { s[r] = dot2bf(xr[r][j], 0x3f803f80u, s[r]); q[r] = dot2bf(xr[r][j], xr[r][j], q[r]); }
  }
#pragma unroll
  for (int j = 0; j < 4; ++j) { t[j] = swap32_add(s[j], s[j + 4]); q[j] = swap32_add(q[j], q[j + 4]); }
#pragma unroll
  for (int u = 0; u < 2; ++u) {
    const float mean = row16_sum(swap16_add(t[u], t[u + 2])) * (1.f / D);
    const float var = rbf(fmaxf(row16_sum(swap16_add(q[u], q[u + 2])) * (1.f / D) - mean * mean, 0.f));
    const unsigned ms = pk_bf16(mean, __builtin_amdgcn_sqrtf(rbf(var + 1e-5f)));
    if ((lane & 15) == 0) __builtin_amdgcn_raw_buffer_store_b64((v2u){ms, tag.x}, rs, WS_GS + (t8 * 8 + u + 2 * (lane >> 4)) * 8, 0, SC1);
    rz[u] = __builtin_amdgcn_rcpf(bfhi(ms));
    mz[u] = -bflo(ms) * rz[u];
  }
  float rr[8], mm[8];
#pragma unroll
  for (int r = 0; r < 8; ++r) { rr[r] = rdlane(rz[r & 1], 16 * (r >> 1)); mm[r] = rdlane(mz[r & 1], 16 * (r >> 1)); }
  STAMP(p, 21);
  v4u o[8];
#pragma unroll
  for (int j = 0; j < 4; ++j) {
    f32x2 v[8];
#pragma unroll
    for (int r = 0; r < 8; ++r) v[r] = __builtin_elementwise_fma((f32x2){bflo(xr[r][j]), bfhi(xr[r][j])}, (f32x2){rr[r], rr[r]}, (f32x2){mm[r], mm[r]});
#pragma unroll
    for (int e = 0; e < 2; ++e) {
#pragma unroll
      for (int i = 0; i < 4; ++i) o[2 * j + e][i] = pk_f16(v[2 * i][e], v[2 * i + 1][e]);
      asm volatile("" : "+v"(o[2 * j + e]));
    }
  }
  STAMP(p, 27);
  bar();
  if (late) { bar(); bar(); }
#pragma unroll
  for (int j = 0; j < 8; ++j) __builtin_amdgcn_raw_buffer_store_b128(o[j], rs, WS_XT + t8 * 8192 + j * 1024 + lane * 16, 0, SC1);
}

DEV void cs_part(const Params& p, LAS char* sm, int cb, int hw, int lane) {
  v4u wr[32];
#pragma unroll
  for (int i = 0; i < 32; ++i) wr[i] = *(const GAS v4u*)((const GAS char*)p.w1 + (size_t)(hw * 256 + i * 8 + (lane >> 3)) * (F * 2) + cb * 128 + (lane & 7) * 16);
  __builtin_amdgcn_sched_barrier(0);
  unsigned sel_lo = 0x00003f80u, sel_hi = 0x3f800000u;
  asm volatile("" : "+s"(sel_lo), "+s"(sel_hi));
  float c[8] = {0.f, 0.f, 0.f, 0.f, 0.f, 0.f, 0.f, 0.f}, t[4], y[2];
#pragma unroll
  for (int i = 0; i < 32; ++i)
#pragma unroll
    for (int j = 0; j < 4; ++j) { c[2 * j] = dot2bf(sel_lo, wr[i][j], c[2 * j]); c[2 * j + 1] = dot2bf(sel_hi, wr[i][j], c[2 * j + 1]); }
#pragma unroll
  for (int j = 0; j < 4; ++j) t[j] = swap32_add(c[j], c[j + 4]);
#pragma unroll
  for (int u = 0; u < 2; ++u) y[u] = dpp_add<0x128>(swap16_add(t[u], t[u + 2]));
  if ((lane & 8) == 0) *(LAS f32x2*)(sm + 65536 + (hw * 64 + (lane & 7) * 8 + 2 * (lane >> 4)) * 4) = (f32x2){y[0], y[1]};
}

struct KPar { unsigned ldt, lar, aim, c0, c1, c2, c3, dsk; };
DEV int chan_of(int b, int ch) { return 16 * (b >> 3) + 8 * ch + (b & 7); }
DEV f32x2 crot(f32x2 w) { return (f32x2){-w.y, w.x}; }
DEV f32x2 cmul(f32x2 a, f32x2 w, f32x2 wr) { return __builtin_elementwise_fma(a.yy, wr, a.xx * w); }
DEV f32x2 cexp_at(float a2, float rv, float pos) {
  const float e = __builtin_amdgcn_exp2f(a2 * pos);
  const float ph = __builtin_amdgcn_fractf(rv * pos);
  return (f32x2){__builtin_amdgcn_cosf(ph), __builtin_amdgcn_sinf(ph)} * e;
}
DEV float exp_acc(float x) {
  const float t = x * 1.44269504088896f;
  const float r = fmaf(x, 1.92596299112661e-8f, fmaf(x, 1.44269504088896f, -t));
  const float e = __builtin_amdgcn_exp2f(t);
  return fmaf(e, r * 0.693147180559945f, e);
}
DEV void kgen(const Params& p, Ts& ts, LAS char* sm, const KPar& k, int w, int lane) {
  const int ch = w >> 1;
  const float dt = rbf(exp_acc(bf2f(k.ldt)));
  const float a2 = -rbf(exp_acc(bf2f(k.lar))) * dt * 1.44269504088896f;
  const float rv = bf2f(k.aim) * dt * 0.159154943091895f;
  LAS char* t = sm + KG_TAB + w * KG_WAVE + lane * 4;
  auto put = [&](int row, f32x2 u) { *(LAS unsigned*)(t + row * KG_ROW) = pk_f16(u.x, u.y); };
  {
    const f32x2 w8 = cexp_at(a2, -rv, 8.f), w8r = crot(w8);
    const f32x2 w64 = cexp_at(a2, -rv, 64.f), w64r = crot(w64), w128 = cmul(w64, w64, w64r);
    f32x2 v[4] = {{1.f, 0.f}, w64, w128, cmul(w128, w64, w64r)};
#pragma unroll
    for (int i = 0; i < 8; ++i)
#pragma unroll
      for (int c = 0; c < 4; ++c) { put(21 + 8 * c + i, v[c]); if (i < 7) v[c] = cmul(v[c], w8, w8r); }
  }
  {
    unsigned mx = max(max(k.c0 & 0x7fffu, k.c1 & 0x7fffu), max(k.c2 & 0x7fffu, k.c3 & 0x7fffu));
    mx = max(mx, (unsigned)__builtin_amdgcn_update_dpp(0, (int)mx, 0xb1, 0xf, 0xf, true));
    mx = max(mx, (unsigned)__builtin_amdgcn_update_dpp(0, (int)mx, 0x4e, 0xf, 0xf, true));
    mx = max(mx, (unsigned)__builtin_amdgcn_update_dpp(0, (int)mx, 0x124, 0xf, 0xf, true));
    mx = max(mx, (unsigned)__builtin_amdgcn_update_dpp(0, (int)mx, 0x128, 0xf, 0xf, true));
    unsigned ex = max(max(__builtin_amdgcn_readlane(mx, 0), __builtin_amdgcn_readlane(mx, 16)), max(__builtin_amdgcn_readlane(mx, 32), __builtin_amdgcn_readlane(mx, 48))) >> 7;
    ex = min(max(ex, 1u), 253u);
    const float sc = __uint_as_float((254u - ex) << 23), isc = __uint_as_float(ex << 23);
    if (lane == 0) *(LAS f32x2*)(sm + SY + 64 + ch * 8) = (f32x2){2.f * dt * isc, bf2f(k.dsk)};
    const f32x2 c = (f32x2){bf2f(k.c0), bf2f(k.c1)} * sc;
    const f32x2 w1 = cexp_at(a2, rv, 1.f), e256 = cexp_at(a2, rv, 256.f), e256r = crot(e256), e512 = cmul(e256, e256, e256r), e1024 = cmul(e512, e512, crot(e512));
    const f32x2 w1r = crot(w1), w2 = cmul(w1, w1, w1r), w2r = crot(w2), w4 = cmul(w2, w2, w2r), w4r = crot(w4), cr = crot(c);
    f32x2 u = c, u4 = cmul(c, w4, w4r), u2 = cmul(e256, c, cr);
#pragma unroll
    for (int i = 0; i < 4; ++i) {
      put(i, u); put(4 + i, u4); put(8 + i, u2);
      if (i < 3) { u = cmul(u, w1, w1r); u4 = cmul(u4, w1, w1r); u2 = cmul(u2, w2, w2r); }
    }
    const f32x2 u512 = cmul(e512, c, cr), u768 = cmul(u512, e256, e256r), u1024 = cmul(e1024, c, cr), u1024r = crot(u1024);
    const f32x2 u1536 = cmul(e512, u1024, u1024r);
    put(12, u512); put(13, cmul(u512, w4, w4r)); put(14, u768); put(15, cmul(u768, w4, w4r));
    put(16, u1024); put(17, cmul(u1024, e256, e256r)); put(18, u1536); put(19, cmul(u1536, e256, e256r)); put(20, cmul(e1024, u1024, u1024r));
  }
  asm volatile("" ::: "memory");
  STAMP(p, 17);
  {
    const int h = lane >> 5, b = lane & 31;
    const LAS char* ua = sm + KG_TAB + w * KG_WAVE + b * KG_ROW + h * 16;
    f32x16 acc = {};
    f16x8 fa[8], fb[8];
#pragma unroll
    for (int s = 0; s < 8; ++s) { fa[s] = *(const LAS f16x8*)(ua + s * 32); fb[s] = *(const LAS f16x8*)(ua + 21 * KG_ROW + s * 32); }
    __builtin_amdgcn_sched_barrier(0);
#pragma unroll
    for (int s = 0; s < 8; ++s) acc = __builtin_amdgcn_mfma_f32_32x32x16_f16(fa[s], fb[s], acc, 0, 0, 0);
    STAMP(p, 18);
    LAS _Float16* kt = (LAS _Float16*)(sm + KG_K + ch * KG_KCH) + 8 + (w & 1);
    auto add = [&](int i, float v) { kt[2 * i] = (_Float16)v; };
    const int i0 = 4 * h + 8 * b, i1 = h ? 384 + 2 * b : 256 + 4 * b, i1s = h ? 64 : 2;
#pragma unroll
    for (int r = 0; r < 4; ++r) add(i0 + r, acc[r]);
    add(i1, acc[4]); add(i1 + 1, acc[5]); add(i1 + i1s, acc[6]); add(i1 + i1s + 1, acc[7]);
    add(512 + 128 * h + b, acc[8]);
    if (h == 0) { add(544 + b, acc[9]); add(576 + b, acc[10]); add(608 + b, acc[11]); }
  }
}
struct KIdx { unsigned a[11]; };
DEV KIdx kexpand_idx(int hw, int lane) {
  KIdx ix;
  const int pp = (hw & 1) * 64 + lane;
#pragma unroll
  for (int i = 0; i < 11; ++i) {
    const int lag = 8 * pp - 3 + i;
    const int code = (int)(pk_bf16((float)lag, 0.f) & 0xffffu) - 0x4280;
    ix.a[i] = KG_K + (hw >> 1) * KG_KCH + 16 + 4 * (lag < 128 ? lag : code);
    asm volatile("" : "+v"(ix.a[i]));
  }
  return ix;
}
DEV void kexpand(LAS char* sm, int hw, int lane, const KIdx& ix) {
  const int ch = hw >> 1;
  const LAS unsigned* kt = (const LAS unsigned*)(sm + KG_K + ch * KG_KCH) + 4;
  LAS char* kc = sm + CV_K + ch * 4 * CV_KS;
  auto sum2 = [&](unsigned hi, unsigned lo) {
    const unsigned a = __builtin_amdgcn_perm(hi, lo, 0x05040100u), b = __builtin_amdgcn_perm(hi, lo, 0x07060302u);
    return __builtin_bit_cast(unsigned, __builtin_bit_cast(f16x2, a) + __builtin_bit_cast(f16x2, b));
  };
  auto emit = [&](int pp, const unsigned (&pr)[10]) {
#pragma unroll
    for (int sg = 0; sg < 4; ++sg) *(LAS v4u*)(kc + sg * CV_KS + (516 - 2 * pp) * 8) = (v4u){pr[6 + sg], pr[4 + sg], pr[2 + sg], pr[sg]};
  };
  const int p3 = 128 + (hw & 1) * 64 + lane;
  const unsigned ka = kt[384 + p3], kb = kt[385 + p3], k0 = kt[p3 == 128 ? 511 : 384 + p3];
  {
    const int pp = (hw & 1) * 64 + lane;
    unsigned kv[12], pr[10], ev[6];
#pragma unroll
    for (int i = 0; i < 11; ++i) kv[i] = *(const LAS unsigned*)(sm + ix.a[i]);
    kv[11] = kv[10];
#pragma unroll
    for (int i = 0; i < 6; ++i) ev[i] = sum2(kv[2 * i], kv[2 * i + 1]);
#pragma unroll
    for (int i = 0; i < 5; ++i) { pr[2 * i] = ev[i]; pr[2 * i + 1] = __builtin_amdgcn_alignbit(ev[i], ev[i + 1], 16); }
    emit(pp, pr);
  }
  {
    const int pp = p3;
    const unsigned ab = sum2(ka, kb);
    const unsigned a0 = sum2(k0, ka);
    const unsigned aa = __builtin_amdgcn_perm(ab, ab, 0x03020302u), bb = __builtin_amdgcn_perm(ab, ab, 0x01000100u);
    const unsigned pr[10] = {a0, aa, aa, aa, aa, aa, (pp & 1) ? ab : aa, (pp & 1) ? bb : ab, bb, bb};
    emit(pp, pr);
  }
  if (lane < 20) *(LAS v4u*)(lane < 4 ? kc + ((hw & 1) * 2 + (lane >> 1)) * CV_KS + (518 + 2 * (lane & 1)) * 8 : sm + CV_X + ch * CV_XS + ((hw & 1) * 16 + lane - 4) * 16) = (v4u){0u, 0u, 0u, 0u};
}

DEV int ks_of(int w, int rl, int i) { const int k = i + (rl >> 3); return 7 * ((w + rl) & 7) + (k >= 7 ? k - 7 : k); }
template <int LO, int HI> DEV void w2_load(rsrc_t rw, int woff, int w, int rl, v4u (&wr)[28]) {
#pragma unroll
  for (int i = LO; i < HI; ++i) wr[i] = __builtin_amdgcn_raw_buffer_load_b128(rw, woff + (i & 3) * 1024, ks_of(w, rl, i >> 2) * 32768, 0);
}
DEV void w2_transpose(v4u* r) {
  unsigned P[8], Q[8];
  asm("s_mov_b64 vcc, %32\n\ts_nop 0\n\t"
#define CN(d, a, b) "v_cndmask_b32_dpp %" #d ", %" #a ", %" #b ", vcc row_ror:8 row_mask:0xf bank_mask:0xf\n\t"
      CN(0, 18, 16) CN(1, 19, 17) CN(2, 22, 20) CN(3, 23, 21) CN(4, 26, 24) CN(5, 27, 25) CN(6, 30, 28) CN(7, 31, 29)
      "s_not_b64 vcc, vcc\n\t"
      CN(8, 16, 18) CN(9, 17, 19) CN(10, 20, 22) CN(11, 21, 23) CN(12, 24, 26) CN(13, 25, 27) CN(14, 28, 30) CN(15, 29, 31)
#undef CN
      : "=&v"(P[0]), "=&v"(P[1]), "=&v"(P[2]), "=&v"(P[3]), "=&v"(P[4]), "=&v"(P[5]), "=&v"(P[6]), "=&v"(P[7]),
        "=&v"(Q[0]), "=&v"(Q[1]), "=&v"(Q[2]), "=&v"(Q[3]), "=&v"(Q[4]), "=&v"(Q[5]), "=&v"(Q[6]), "=&v"(Q[7])
      : "v"(r[0][0]), "v"(r[0][1]), "v"(r[0][2]), "v"(r[0][3]), "v"(r[1][0]), "v"(r[1][1]), "v"(r[1][2]), "v"(r[1][3]),
        "v"(r[2][0]), "v"(r[2][1]), "v"(r[2][2]), "v"(r[2][3]), "v"(r[3][0]), "v"(r[3][1]), "v"(r[3][2]), "v"(r[3][3]), "s"(0x00ff00ff00ff00ffull)
      : "vcc", "scc");
#pragma unroll
  for (int t = 0; t < 4; ++t) {
    const unsigned sel = (t & 1) ? 0x07060302u : 0x05040100u;
    const int u = t >> 1;
    r[t] = (v4u){__builtin_amdgcn_perm(P[2 + u], P[u], sel), __builtin_amdgcn_perm(P[6 + u], P[4 + u], sel),
                 __builtin_amdgcn_perm(Q[2 + u], Q[u], sel), __builtin_amdgcn_perm(Q[6 + u], Q[4 + u], sel)};
  }
  asm volatile("" : "+v"(r[0]), "+v"(r[1]), "+v"(r[2]), "+v"(r[3]));
}

struct Loader { const char* gb; int cstep, gstep, rot; unsigned slot, v[4]; };
DEV Loader loader_init(const Params& p, LAS char* sm, int rt, int ct, int rot, int hw, int lane) {
  Loader ld;
  ld.gb = hw ? (const char*)p.w1 + ct * 256 : (const char*)p.x + (size_t)rt * 128 * 1024;
  ld.cstep = hw ? 64 * F * 2 : 128;
  ld.gstep = hw ? 16 * F * 2 : 32768;
  ld.rot = rot;
  ld.slot = (unsigned)(size_t)sm + hw * 16384;
#pragma unroll
  for (int i = 0; i < 4; ++i) {
    const int rx = i * 8 + (lane >> 3), rw = i * 4 + (lane >> 4);
    ld.v[i] = hw ? rw * (F * 2) + (((lane & 15) ^ ((rw & 3) << 2) ^ (i & 2)) << 4) : rx * 1024 + (((lane & 7) ^ ((rx >> 1) & 7)) << 4);
  }
  return ld;
}
DEV void loader_issue(const Loader& ld, int c) {
  const char* g0 = ld.gb + (size_t)(((c + ld.rot) & 7) * ld.cstep);
  const char *g1 = g0 + ld.gstep, *g2 = g1 + ld.gstep, *g3 = g2 + ld.gstep;
#define GL4(S) "global_load_lds_dwordx4 %0, " S "\n\ts_add_u32 m0, m0, 0x400\n\ts_nop 0\n\tglobal_load_lds_dwordx4 %1, " S "\n\ts_add_u32 m0, m0, 0x400\n\ts_nop 0\n\t" \
               "global_load_lds_dwordx4 %2, " S "\n\ts_add_u32 m0, m0, 0x400\n\ts_nop 0\n\tglobal_load_lds_dwordx4 %3, " S "\n\ts_add_u32 m0, m0, 0x400\n\ts_nop 0\n\t"
  asm volatile("s_mov_b32 m0, %8\n\ts_nop 0\n\t" GL4("%4") GL4("%5") GL4("%6") GL4("%7")
               :: "v"(ld.v[0]), "v"(ld.v[1]), "v"(ld.v[2]), "v"(ld.v[3]), "s"(g0), "s"(g1), "s"(g2), "s"(g3), "s"(ld.slot + (c & 3) * G1_SLOT) : "memory", "scc");
#undef GL4
}
struct G1Frag { v4u a[4], b[4]; };
DEV void g1_main(const Params& p, Ts& ts, LAS char* sm, rsrc_t rs, int rt, int ct, int w, int lane, v2u tag) {
  const int wf = w & 1, wt = w >> 1, g = lane >> 4, r16 = lane & 15;
  f32x4 acc[4][4] = {};
  const int q4 = r16 >> 2, p4 = lane & 3;
  int aoff[2], boff[2];
#pragma unroll
  for (int i = 0; i < 2; ++i) aoff[i] = 16384 + (8 * g + q4) * 256 + (((8 * wf + 4 * i + (p4 >> 1)) ^ (q4 << 2) ^ ((g & 1) << 1)) << 4) + 8 * (p4 & 1);
  const int tok0 = wt * 64 + r16;
#pragma unroll
  for (int s = 0; s < 2; ++s) boff[s] = tok0 * 128 + (((4 * s + g) ^ ((tok0 >> 1) & 7)) << 4);
  auto rd = [&](int c, int s) {
    G1Frag f;
    LAS char* base = sm + (c % G1_R) * G1_SLOT;
#pragma unroll
    for (int i = 0; i < 4; ++i) {
      const int o = (aoff[i >> 1] ^ ((i & 1) << 5)) + s * 8192;
      const v2u a0 = __builtin_bit_cast(v2u, __builtin_amdgcn_ds_read_tr16_b64_v4i16((LAS s16x4*)(base + o)));
      const v2u a1 = __builtin_bit_cast(v2u, __builtin_amdgcn_ds_read_tr16_b64_v4i16((LAS s16x4*)(base + o + 1024)));
      f.a[i] = (v4u){a0.x, a0.y, a1.x, a1.y};
      f.b[i] = *(const LAS v4u*)(base + boff[s] + 2048 * i);
    }
    return f;
  };
  G1Frag fr[16];
#pragma unroll
  for (int st = -1; st < 16; ++st) {
    const int ld = st + 1;
    if (ld < 16) {
      if ((ld & 1) == 0 && ld < 14) lds_barrier();
      if (ld == 0) STAMP(p, 19);
      if (ld == 2) STAMP(p, 20);
      if (ld == 4) STAMP(p, 24);
      if (ld == 8) STAMP(p, 25);
      if (ld == 12) STAMP(p, 26);
      fr[ld] = rd(ld >> 1, ld & 1);
    }
    if (st >= 0) {
#pragma unroll
      for (int i = 0; i < 4; ++i)
#pragma unroll
        for (int j = 0; j < 4; ++j)
          acc[i][j] = __builtin_amdgcn_mfma_f32_16x16x32_bf16(__builtin_bit_cast(bf16x8, fr[st].a[i]), __builtin_bit_cast(bf16x8, fr[st].b[j]), acc[i][j], 0, 0, 0);
#pragma unroll
      for (int i = 0; i < 12; ++i) {
        __builtin_amdgcn_sched_group_barrier(0x8, 1, 0);
        __builtin_amdgcn_sched_group_barrier(0x100, 1, 0);
      }
      __builtin_amdgcn_sched_group_barrier(0x8, 4, 0);
    }
    __builtin_amdgcn_sched_barrier(0);
  }
  STAMP(p, 3);
  __builtin_amdgcn_s_setprio(1);
  lds_wait(sm, 8);
  unsigned gs[4], gc[4];
#pragma unroll
  for (int i = 0; i < 4; ++i) {
    gs[i] = *(const LAS unsigned*)(sm + GR + (tok0 + 16 * i) * 4);
    gc[i] = *(const LAS unsigned*)(sm + GR + 512 + (wf * 64 + r16 + 16 * i) * 4);
  }
  STAMP(p, 30);
  float c1[4], sd[4];
#pragma unroll
  for (int j = 0; j < 4; ++j) {
    const unsigned m2 = __builtin_amdgcn_perm(gs[j], gs[j], 0x01000100u) ^ 0x80008000u;
    const v2u bm = {g == 0 ? m2 : 0u, 0u};
#pragma unroll
    for (int i = 0; i < 4; ++i)
      acc[i][j] = __builtin_amdgcn_mfma_f32_16x16x16bf16_1k(__builtin_bit_cast(s16x4, (v2u){gc[i], 0u}), __builtin_bit_cast(s16x4, bm), acc[i][j], 0, 0, 0);
    sd[j] = bfhi(gs[j]);
    c1[j] = __builtin_amdgcn_rcpf(sd[j]) * -1.44269504088896f;
  }
  const int hbase = WS_HF + ((2 * rt + wt) * 56 + 4 * ct + 2 * wf) * 4096 + lane * 16;
#pragma unroll
  for (int m = 0; m < 2; ++m)
#pragma unroll
    for (int j = 0; j < 4; ++j) {
      v4u o;
#pragma unroll
      for (int e = 0; e < 4; ++e) {
        const f32x4 a4 = acc[2 * m + (e >> 1)][j];
        const f32x2 t = (e & 1) ? a4.zw : a4.xy;
        const f32x2 u = t * c1[j];
        const f32x2 ex = {__builtin_amdgcn_exp2f(u.x), __builtin_amdgcn_exp2f(u.y)};
        const f32x2 dn = __builtin_elementwise_fma(ex, (f32x2){sd[j], sd[j]}, (f32x2){sd[j], sd[j]});
        const f32x2 r = t * (f32x2){__builtin_amdgcn_rcpf(dn.x), __builtin_amdgcn_rcpf(dn.y)};
        o[e] = pk_bf16(r.x, r.y);
      }
      __builtin_amdgcn_raw_buffer_store_b128(o, rs, hbase + m * 4096 + j * 1024, 0, SC1);
      __builtin_amdgcn_sched_barrier(0);
    }
  STAMP(p, 4);
  lds_set(sm, 12 + w);
  publish(rs, sm, 1, 4, WS_FH + (rt * 16 + ct) * FSTR, tag, lane);
  __builtin_amdgcn_s_setprio(0);
}

DEV void conv(const Params& p, Ts& ts, LAS char* sm, rsrc_t rs, int b, int hw, int lane, v2u tag, v4u (&wr)[28]) {
  const int ch = hw >> 1, eh = hw & 1, g = lane >> 4, r16 = lane & 15, d = chan_of(b, ch);
  LAS char* xs = sm + CV_X + ch * CV_XS;
  lds_wait(sm, 4, 4u);
  STAMP(p, 6);
  const LAS char* ka = sm + CV_K + (ch * 4 + (r16 & 3)) * CV_KS + (517 - (r16 >> 2) + 2 * g) * 8;
  const LAS char* xa = xs + 512 + r16 * 32 + g * 16;
  auto lda = [&](const LAS char* ptr) {
    const v2u lo = *(const volatile LAS v2u*)ptr, hi = *(const volatile LAS v2u*)(ptr + 8);
    return __builtin_bit_cast(f16x8, (v4u){lo.x, lo.y, hi.x, hi.y});
  };
  f32x4 acc[8];
#pragma unroll
  for (int a = 0; a < 8; ++a) acc[a] = (f32x4){0.f, 0.f, 0.f, 0.f};
  const LAS char* ke = ka - 256 * eh - 3584 - 256;
  const LAS char* xe = xa - 256 * eh - 256;
  f16x8 A[4][8], B[4][8];
  auto ld = [&](int e, int i) { if (e < 4) { A[e][i] = lda(ke + 3584 - 512 * i + 224 - 64 * e); B[e][i] = *(const LAS f16x8*)(xe + 224 - 64 * e + 512 * i); } };
  ld(0, 0); ld(0, 7); ld(0, 1); ld(0, 6); ld(0, 2); ld(0, 5); ld(0, 3); ld(0, 4);
  __builtin_amdgcn_sched_barrier(0);
  static_for<0, 4>([&](auto ee) {
    constexpr int e = decltype(ee)::v;
    static_for<0, 8>([&](auto ll) {
      constexpr int lv = 7 - decltype(ll)::v;
      constexpr int n = (7 - lv < lv ? 7 - lv : lv - 1) + 1;
      if constexpr (lv == 4) ld(e + 1, 0);
      if constexpr (lv == 3) ld(e + 1, 1);
      if constexpr (lv < 7 && lv >= 1) ld(e + 1, lv + 1);
#pragma unroll
      for (int i = 0; i < n; ++i) {
        acc[lv + i] = __builtin_amdgcn_mfma_f32_16x16x32_f16(A[e][i], B[e][lv], acc[lv + i], 0, 0, 0);
        acc[lv + i] = __builtin_amdgcn_mfma_f32_16x16x32_f16(A[e][lv], B[e][i], acc[lv + i], 0, 0, 0);
      }
      if constexpr (2 * lv <= 7) acc[2 * lv] = __builtin_amdgcn_mfma_f32_16x16x32_f16(A[e][lv], B[e][lv], acc[2 * lv], 0, 0, 0);
#pragma unroll
      for (int i = 0; i < 6; ++i) { __builtin_amdgcn_sched_group_barrier(0x8, 1, 0); __builtin_amdgcn_sched_group_barrier(0x100, 1, 0); }
      __builtin_amdgcn_sched_barrier(0);
    });
  });
  __builtin_amdgcn_s_setprio(2);
  LAS f32x4* ex = (LAS f32x4*)(sm + CV_E);
  const f32x2 md = *(const LAS f32x2*)(sm + SY + 64 + ch * 8);
  const int t0 = 1024 * eh + 16 * r16 + 4 * g;
  v2u xr[4];
#pragma unroll
  for (int i = 0; i < 4; ++i) xr[i] = *(const LAS v2u*)(xs + 512 + (t0 + 256 * i) * 2);
  f32x4 own[4];
  if (eh) {
#pragma unroll
    for (int i = 0; i < 4; ++i) { ex[(hw * 4 + i) * 64 + lane] = acc[i]; own[i] = acc[4 + i]; }
    asm volatile("; tiles 4..7" ::: "memory");
  } else {
#pragma unroll
    for (int i = 0; i < 4; ++i) { ex[(hw * 4 + i) * 64 + lane] = acc[4 + i]; own[i] = acc[i]; }
    asm volatile("; tiles 0..3" ::: "memory");
  }
  asm volatile("s_waitcnt lgkmcnt(0)" ::: "memory");
  if (lane == 0) __hip_atomic_fetch_add((LAS unsigned*)(sm + SY) + 6 + ch, 1u, __ATOMIC_RELAXED, __HIP_MEMORY_SCOPE_WORKGROUP);
  const float mult = rdlane(md.x, 0), dsk = rdlane(md.y, 0);
#pragma unroll
  for (int i = 0; i < 4; ++i) {
    const unsigned xr0 = xr[i].x, xr1 = xr[i].y;
    const f16x2 x0 = __builtin_bit_cast(f16x2, xr0), x1 = __builtin_bit_cast(f16x2, xr1);
    own[i] = own[i] * mult + (f32x4){(float)x0.x, (float)x0.y, (float)x1.x, (float)x1.y} * dsk;
    asm volatile("" : "+v"(own[i]));
  }
  STAMP(p, 7);
  lds_wait(sm, 6 + ch, 2u);
#pragma unroll
  for (int i = 0; i < 4; ++i) {
    const f32x4 y = ex[((hw ^ 1) * 4 + i) * 64 + lane] * mult + own[i];
    __builtin_amdgcn_raw_buffer_store_b64((v2u){pk_bf16(y[0], y[1]), pk_bf16(y[2], y[3])}, rs, WS_ST + d * 4096 + (t0 + 256 * i) * 2, 0, SC1);
  }
  STAMP(p, 5);
  publish(rs, sm, 2, 4, WS_FS + b * FSTR, tag, lane);
  __builtin_amdgcn_s_setprio(0);
}

template <bool MAIN> DEV void gemm2(const Params& p, Ts& ts, LAS char* sm, rsrc_t rs, int rt2, int ct2, int rl, bool isL, int w, int lane, v2u tag, v4u (&wr)[28]) {
  const int g = lane >> 4, hb = (lane >> 3) & 1, cb = lane & 7;
  STAMP(p, 31);
  if (MAIN && w == 0) { wait_flags(rs, WS_FH + (rt2 >> 1) * 16 * FSTR, 14, tag, lane); lds_set(sm, 5); }
  else lds_wait(sm, 5);
  STAMP(p, 10);
  v4u ha[28];
  const int hoff = WS_HF + rt2 * 56 * 4096 + lane * 16;
  auto hload = [&](int i) { return __builtin_amdgcn_raw_buffer_load_b128(rs, hoff + (i & 3) * 1024, ks_of(w, rl, i >> 2) * 4096, SC1); };
#pragma unroll
  for (int i = 0; i < 12; ++i) ha[i] = hload(i);
  __builtin_amdgcn_sched_barrier(0);
  f32x4 acc[4][4] = {};
  auto mm = [&](int ks, int mt) {
#pragma unroll
    for (int t = 0; t < 4; ++t)
      acc[mt][t] = __builtin_amdgcn_mfma_f32_16x16x32_bf16(__builtin_bit_cast(bf16x8, ha[4 * ks + mt]), __builtin_bit_cast(bf16x8, wr[4 * ks + t]), acc[mt][t], 0, 0, 0);
  };
#pragma unroll
  for (int ks = 0; ks < 4; ++ks) {
#pragma unroll
    for (int mt = 0; mt < 4; ++mt) mm(ks, mt);
    __builtin_amdgcn_sched_barrier(0);
    if (ks == 0) {
#pragma unroll
      for (int i = 12; i < 16; ++i) ha[i] = hload(i);
    }
    if (ks == 1 || ks == 2) {
#pragma unroll
      for (int i = 0; i < 6; ++i) { const int e = 6 * (ks - 1) + i; ha[16 + 4 * (e % 3) + e / 3] = hload(16 + 4 * (e % 3) + e / 3); }
    }
    if (!MAIN || 3 + ks >= MAIN_PRE_T) w2_transpose(wr + 12 + 4 * ks);
    __builtin_amdgcn_sched_barrier(0);
  }
  lds_wait(sm, MAIN ? 1 : 2, MAIN && isL ? 0u : 4u);
  LAS f32x4* red = (LAS f32x4*)(sm + (w ^ 4) * 16384);
#pragma unroll
  for (int mt = 0; mt < 4; ++mt) {
#pragma unroll
    for (int ks = 4; ks < 7; ++ks) mm(ks, mt);
    if (mt > 0) {
#pragma unroll
      for (int t = 0; t < 4; ++t) red[((mt - 1) * 4 + t) * 64 + lane] = acc[mt - 1][t];
    }
#pragma unroll
    for (int i = 0; i < 4; ++i) { __builtin_amdgcn_sched_group_barrier(0x8, 3, 0); __builtin_amdgcn_sched_group_barrier(0x200, 1, 0); }
    __builtin_amdgcn_sched_barrier(0);
  }
#pragma unroll
  for (int t = 0; t < 4; ++t) red[(12 + t) * 64 + lane] = acc[3][t];
  STAMP(p, 11);
  const int tok = rt2 * 64 + 16 * w + 4 * g, chn = ct2 * 64 + 8 * cb + 4 * hb;
  v2u xv[4];
  f32x4 o[4], sf[4], xf[4];
  if (MAIN) {
#pragma unroll
    for (int i = 0; i < 4; ++i) xv[i] = *(const GAS v2u*)((const GAS char*)p.x + (size_t)(tok + i) * 1024 + chn * 2);
    if (w == 0) {
      lds_wait(sm, 2, 4u);
      wait_flags(rs, WS_FS + ct2 * 32 * FSTR, 32, tag, lane);
      lds_set(sm, 3);
    } else lds_wait(sm, 3);
#pragma unroll
    for (int i = 0; i < 2; ++i) {
      const int c = 16 * w + 8 * i + (lane >> 3);
      *(LAS v4u*)(sm + G2_S + c * 144 + (lane & 7) * 16) =
          __builtin_amdgcn_raw_buffer_load_b128(rs, WS_ST + (ct2 * 64 + c) * 4096 + rt2 * 128 + (lane & 7) * 16, 0, SC1);
    }
    wave_sync(sm, 10, 4);
#pragma unroll
    for (int t = 0; t < 4; ++t) {
      const LAS f32x4* r0 = (const LAS f32x4*)(sm + 65536) + (w * 4 + t) * 64 + lane;
      o[t] = (r0[0] + r0[1024]) + (r0[2048] + r0[3072]);
      const v2u sv = *(const LAS v2u*)(sm + G2_S + (8 * cb + 4 * hb + t) * 144 + 32 * w + 8 * g);
      sf[t] = (f32x4){bflo(sv.x), bfhi(sv.x), bflo(sv.y), bfhi(sv.y)};
      xf[t] = (f32x4){(t & 1) ? bfhi(xv[0][t >> 1]) : bflo(xv[0][t >> 1]), (t & 1) ? bfhi(xv[1][t >> 1]) : bflo(xv[1][t >> 1]),
                      (t & 1) ? bfhi(xv[2][t >> 1]) : bflo(xv[2][t >> 1]), (t & 1) ? bfhi(xv[3][t >> 1]) : bflo(xv[3][t >> 1])};
      asm volatile("" : "+v"(o[t]), "+v"(sf[t]), "+v"(xf[t]));
    }
  }
  STAMP(p, 28);
  lds_barrier();
  STAMP(p, 12);
  if (MAIN) {
    const rsrc_t ro = __builtin_amdgcn_make_buffer_rsrc(p.out, 0, L * D * 2, 0x00020000);
#pragma unroll
    for (int t = 0; t < 4; ++t) {
      const LAS f32x4* r0 = (const LAS f32x4*)sm + (w * 4 + t) * 64 + lane;
      o[t] += (r0[0] + r0[1024]) + (r0[2048] + r0[3072]);
      const f32x4 u = o[t] * -1.44269504088896f;
      const f32x4 dn = (f32x4){__builtin_amdgcn_exp2f(u[0]), __builtin_amdgcn_exp2f(u[1]), __builtin_amdgcn_exp2f(u[2]), __builtin_amdgcn_exp2f(u[3])} + 1.f;
      const f32x4 rc = {__builtin_amdgcn_rcpf(dn[0]), __builtin_amdgcn_rcpf(dn[1]), __builtin_amdgcn_rcpf(dn[2]), __builtin_amdgcn_rcpf(dn[3])};
      o[t] = __builtin_elementwise_fma(o[t] * rc, sf[t], xf[t]);
    }
#pragma unroll
    for (int i = 0; i < 4; ++i)
      __builtin_amdgcn_raw_buffer_store_b64((v2u){pk_bf16(o[0][i], o[1][i]), pk_bf16(o[2][i], o[3][i])}, ro, (tok + i) * 1024 + chn * 2, 0, OUT_AUX);
  }
  STAMP(p, 13);
}

__global__ void __launch_bounds__(512, 2) fused_kernel(Params p) {
  __shared__ __attribute__((aligned(16))) char smem[LDS_BYTES];
  LAS char* sm = (LAS char*)smem;
  const int tid = threadIdx.x, lane = tid & 63, w = __builtin_amdgcn_readfirstlane(tid >> 6);
  const int b = blockIdx.x, xcd = b & 7, idx = b >> 3;
  const bool isL = idx < 4;
  const int ii = idx - 4, rt = (xcd & 3) * 4 + (ii & 3), ct = (xcd >> 2) * 7 + (ii >> 2);
  const int rt2 = (xcd & 3) * 8 + (xcd >> 2) * 4 + (idx >> 3), ct2 = idx & 7;
  const int rl = ((2 * (idx >> 3) + 4 * (ct2 & 1)) & 7) + 8 * (4 * (((ct2 >> 1) ^ xcd) & 1));
  const int t8 = (xcd & 3) * 64 + (xcd >> 2) * 32 + idx;
  Ts ts = {};
  STAMP(p, 0);
  unsigned pf;
  asm volatile("global_load_dword %0, %1, %2" : "=v"(pf) : "v"(min((((idx * 8 + w) ^ ((lane & 3) << 7)) << 7), CODE_BYTES - 128)), "s"(__builtin_amdgcn_s_getpc() & ~(u64)255));
  __builtin_amdgcn_sched_barrier(0);
  const rsrc_t rs = __builtin_amdgcn_make_buffer_rsrc(p.ws, 0, WS_BYTES, 0x00020000);
  asm volatile("" ::"s"(p.x), "s"(p.lar), "s"(p.aim), "s"(p.cre), "s"(p.cim), "s"(p.ldt), "s"(p.dsk), "s"(p.w1), "s"(p.w2), "s"(p.ws));
  v2u ep;
  KPar kp;
  v4u early[8];
  asm volatile("global_load_dwordx2 %0, %1, %2 sc1" : "=v"(ep) : "v"(WS_EPOCH), "s"(p.ws));
  if (w < 4) {
    const unsigned d2 = chan_of(b, w >> 1) * 2, o = d2 * NM + ((w & 1) * 64 + lane) * 2, o2 = o ^ 128;
    asm volatile("global_load_ushort %0, %8, %11\n\tglobal_load_ushort %1, %9, %12\n\tglobal_load_ushort %2, %9, %13\n\t"
                 "global_load_ushort %3, %9, %14\n\tglobal_load_ushort %4, %9, %15\n\tglobal_load_ushort %5, %10, %14\n\t"
                 "global_load_ushort %6, %10, %15\n\tglobal_load_ushort %7, %8, %16"
                 : "=&v"(kp.ldt), "=&v"(kp.lar), "=&v"(kp.aim), "=&v"(kp.c0), "=&v"(kp.c1), "=&v"(kp.c2), "=&v"(kp.c3), "=&v"(kp.dsk)
                 : "v"(d2), "v"(o), "v"(o2), "s"(p.ldt), "s"(p.lar), "s"(p.aim), "s"(p.cre), "s"(p.cim), "s"(p.dsk));
  }
  if (w == 6) {
    const char* sb = (const char*)p.x + (size_t)t8 * 8192;
    const unsigned o = lane * 16, o2 = o + 4096;
    asm volatile("global_load_dwordx4 %0, %8, %10\n\tglobal_load_dwordx4 %1, %8, %10 offset:1024\n\tglobal_load_dwordx4 %2, %8, %10 offset:2048\n\t"
                 "global_load_dwordx4 %3, %8, %10 offset:3072\n\tglobal_load_dwordx4 %4, %9, %10\n\tglobal_load_dwordx4 %5, %9, %10 offset:1024\n\t"
                 "global_load_dwordx4 %6, %9, %10 offset:2048\n\tglobal_load_dwordx4 %7, %9, %10 offset:3072"
                 : "=&v"(early[0]), "=&v"(early[1]), "=&v"(early[2]), "=&v"(early[3]), "=&v"(early[4]), "=&v"(early[5]), "=&v"(early[6]), "=&v"(early[7])
                 : "v"(o), "v"(o2), "s"(sb));
  }
  KIdx kix;
  if (w >= 4) kix = kexpand_idx(w - 4, lane);
  Loader ld;
  if ((w & 6) == 4 && !isL) {
    ld = loader_init(p, sm, rt, ct, 2 * (ii & 3) + 2 * (ii >> 2), w - 4, lane);
    __builtin_amdgcn_s_sleep(LOADER_DELAY);
#pragma unroll 1
    for (int c = 0; c < 3; ++c) loader_issue(ld, c);
    STAMP(p, 2);
  }
  __builtin_amdgcn_sched_barrier(0);
  const rsrc_t rw = __builtin_amdgcn_make_buffer_rsrc((void*)p.w2, 0, F * D * 2, 0x00020000);
  const int woff = (16 * ((lane >> 3) & 1) + 4 * (lane >> 4)) * 1024 + ct2 * 128 + (lane & 7) * 16;
  auto make_tag = [&]() {
    unsigned lo, hi;
    asm volatile("v_readfirstlane_b32 %0, %2\n\tv_readfirstlane_b32 %1, %3" : "=s"(lo), "=s"(hi) : "v"(ep.x), "v"(ep.y), "v"(pf));
    const u64 t = (((u64)hi << 32) | lo) + 1ull;
    return (v2u){(unsigned)t, (unsigned)(t >> 32)};
  };
  auto finish = [&]() {
    __builtin_amdgcn_endpgm();
  };
  if (w < 4) {
    __builtin_amdgcn_s_setprio(1);
    if (w == 0 && lane < 16) *(LAS unsigned*)(sm + SY + lane * 4) = 0u;
    if (w == 1 && lane < 8) *(LAS unsigned*)(sm + KG_K + (lane >> 2) * KG_KCH + (lane & 3) * 4) = 0u;
    __builtin_amdgcn_sched_barrier(0);
    drain();
    __builtin_amdgcn_sched_barrier(0);
    asm volatile("" : "+v"(kp.ldt), "+v"(kp.lar), "+v"(kp.aim), "+v"(kp.c0), "+v"(kp.c1), "+v"(kp.c2), "+v"(kp.c3), "+v"(kp.dsk));
    const v2u tag = make_tag();
    STAMP(p, 1);
    kgen(p, ts, sm, kp, w, lane);
    STAMP(p, 2);
    if (isL) {
      asm volatile("s_waitcnt lgkmcnt(0)" ::: "memory");
#pragma unroll 1
      for (int i = 0; i < 7; ++i) bar();
      __builtin_amdgcn_s_setprio(0);
    } else g1_main(p, ts, sm, rs, rt, ct, w, lane, tag);
    STAMP(p, 5);
    v4u wr[28];
    w2_load<0, 28>(rw, woff, w, rl, wr);
    __builtin_amdgcn_sched_barrier(0);
    STAMP(p, 23);
#pragma unroll
    for (int ks = 0; ks < MAIN_PRE_T; ++ks) { w2_transpose(wr + 4 * ks); __builtin_amdgcn_sched_barrier(0); }
    gemm2<true>(p, ts, sm, rs, rt2, ct2, rl, isL, w, lane, tag, wr);
    finish();
  } else {
    v2u tag, f[4];
    int poff, pst1, pst2;
    if (w >= 6) {
      drain();
      __builtin_amdgcn_sched_barrier(0);
      tag = make_tag();
      STAMP(p, 1);
      if (w == 6) {
        asm volatile("" : "+v"(early[0]), "+v"(early[1]), "+v"(early[2]), "+v"(early[3]), "+v"(early[4]), "+v"(early[5]), "+v"(early[6]), "+v"(early[7]));
        ln_wave(p, ts, rs, t8, lane, tag, !isL, early);
        if (!isL) bar();
        drain();
        if (lane == 0) __builtin_amdgcn_raw_buffer_store_b64(tag, rs, WS_FX + b * FSTR, 0, SC1);
        STAMP(p, 22);
        if (isL) { bar(); bar(); bar(); }
      } else { bar(); bar(); bar(); bar(); }
      bar(); bar();
      poff = w == 7 ? WS_FX + lane * FSTR : WS_GS + (isL ? 0 : rt * 1024) + lane * 8;
      pst1 = w == 7 ? 64 * FSTR : 512;
      pst2 = w == 7 ? 128 * FSTR : WS_GC - WS_GS + (isL ? 0 : (ct - rt) * 1024);
      if (w == 6) {
#pragma unroll
        for (int i = 0; i < 4; ++i) f[i] = __builtin_amdgcn_raw_buffer_load_b64(rs, poff, (i & 1) * pst1 + (i >> 1) * pst2, SC1);
      }
    } else if (isL) {
      const int cg = (xcd & 3) * 4 + idx;
      cs_part(p, sm, (xcd >> 2) * 14 + min(cg, 13), w - 4, lane);
      lds_barrier();
      STAMP(p, 16);
      if (w == 4 && cg < 14) {
        const float cs = *(const LAS float*)(sm + 65536 + lane * 4) + *(const LAS float*)(sm + 65536 + 256 + lane * 4);
        const float hi = rbf(cs);
        __builtin_amdgcn_raw_buffer_store_b64((v2u){pk_bf16(hi, cs - hi), make_tag().x}, rs, WS_GC + (((xcd >> 2) * 14 + cg) * 64 + lane) * 8, 0, SC1);
      }
#pragma unroll 1
      for (int i = 0; i < 5; ++i) bar();
    }
    __builtin_amdgcn_sched_barrier(0);
    v4u wr[28];
    w2_load<0, W2_PRE>(rw, woff, w, rl, wr);
    __builtin_amdgcn_sched_barrier(0);
    STAMP(p, 3);
    auto poll4 = [&](unsigned tx, unsigned mx) {
#pragma unroll 1
      for (unsigned spins = 0; spins < SPIN_CAP; ++spins) {
        asm volatile("" ::: "memory");
#pragma unroll
        for (int i = 0; i < 4; ++i) f[i] = __builtin_amdgcn_raw_buffer_load_b64(rs, poff, (i & 1) * pst1 + (i >> 1) * pst2, SC1);
        unsigned bad = 0;
#pragma unroll
        for (int i = 0; i < 4; ++i) bad |= (f[i].y ^ tx) | ((f[i].x ^ tag.x) & mx);
        if (__all(bad == 0u)) break;
        __builtin_amdgcn_s_sleep(4);
      }
      __builtin_amdgcn_fence(__ATOMIC_ACQUIRE, "wavefront");
    };
    if (w >= 6) {
      bar();
      if (w == 6) {
        if (!__all(((f[0].y ^ tag.x) | (f[1].y ^ tag.x) | (f[2].y ^ tag.x) | (f[3].y ^ tag.x)) == 0u)) poll4(tag.x, 0u);
#pragma unroll
        for (int i = 0; i < 4; ++i) *(LAS unsigned*)(sm + GR + i * 256 + lane * 4) = f[i].x;
        asm volatile("" ::: "memory");
        lds_set(sm, 8);
      }
    } else {
      if (isL) bar();
      else {
#pragma unroll 1
        for (int c = 2; c < 8; ++c) {
          if (c > 2) loader_issue(ld, c);
          if (c < 5) asm volatile("s_waitcnt vmcnt(%0)" ::"n"(32 + W2_PRE) : "memory");
          else asm volatile("s_waitcnt vmcnt(32)" ::: "memory");
          __builtin_amdgcn_s_barrier();
        }
        drain();
        __builtin_amdgcn_s_barrier();
      }
      tag = make_tag();
    }
    __builtin_amdgcn_sched_barrier(0);
    STAMP(p, 9);
    w2_load<W2_PRE, 28>(rw, woff, w, rl, wr);
    __builtin_amdgcn_sched_barrier(0);
    STAMP(p, 4);
    v4u xv[8];
    if (w == 7) {
      poll4(tag.y, ~0u);
      STAMP(p, 14);
#pragma unroll
      for (int i = 0; i < 8; ++i)
        xv[i] = __builtin_amdgcn_raw_buffer_load_b128(rs, WS_XT + (lane >> 1) * 8192 + (b & 7) * 1024 + (2 * (b >> 3) + (lane & 1)) * 16, i * 32 * 8192, SC1);
      if (b == 0 && lane == 0) __builtin_amdgcn_raw_buffer_store_b64(tag, rs, WS_EPOCH, 0, SC1);
    }
    __builtin_amdgcn_sched_barrier(0);
    __builtin_amdgcn_s_setprio(EXP_PRIO);
    kexpand(sm, w - 4, lane, kix);
    __builtin_amdgcn_sched_barrier(0);
    STAMP(p, 15);
    if (w == 7) {
#pragma unroll
      for (int i = 0; i < 8; ++i) *(LAS v4u*)(sm + CV_X + (lane & 1) * CV_XS + 512 + (i * 32 + (lane >> 1)) * 16) = xv[i];
    }
    __builtin_amdgcn_s_setprio(0);
    asm volatile("s_waitcnt lgkmcnt(0)" ::: "memory");
    if (lane == 0) __hip_atomic_fetch_add((LAS unsigned*)(sm + SY) + 4, 1u, __ATOMIC_RELAXED, __HIP_MEMORY_SCOPE_WORKGROUP);
    __builtin_amdgcn_sched_barrier(0);
    w2_transpose(wr); w2_transpose(wr + 4); w2_transpose(wr + 8);
    __builtin_amdgcn_sched_barrier(0);
    STAMP(p, 29);
    if (!isL) lds_wait(sm, 8 + w);
    conv(p, ts, sm, rs, b, w - 4, lane, tag, wr);
    STAMP(p, 8);
    gemm2<false>(p, ts, sm, rs, rt2, ct2, rl, isL, w, lane, tag, wr);
    finish();
  }
}

extern "C" void kernel_launch(void* const* d_in, const int* in_sizes, int n_in, void* d_out, int out_size, void* d_ws, size_t ws_size,
                              hipStream_t stream) {
  Params p;
  p.x = (const u16*)d_in[0]; p.lar = (const u16*)d_in[1]; p.aim = (const u16*)d_in[2]; p.cre = (const u16*)d_in[3];
  p.cim = (const u16*)d_in[4]; p.ldt = (const u16*)d_in[5]; p.dsk = (const u16*)d_in[6]; p.w1 = (const u16*)d_in[8];
  p.w2 = (const u16*)d_in[9]; p.out = (u16*)d_out; p.ws = (char*)d_ws;
  fused_kernel<<<256, 512, 0, stream>>>(p);
}
```

```cpp
#include <hip/hip_runtime.h>
#include <hip/hip_bf16.h>
#include <stdint.h>

typedef unsigned short u16;
typedef unsigned long long u64;
typedef unsigned v4u __attribute__((ext_vector_type(4)));
typedef unsigned v2u __attribute__((ext_vector_type(2)));
typedef float f32x2 __attribute__((ext_vector_type(2)));
typedef float f32x4 __attribute__((ext_vector_type(4)));
typedef float f32x16 __attribute__((ext_vector_type(16)));
typedef short s16x4 __attribute__((ext_vector_type(4)));
typedef __bf16 bf16x2 __attribute__((ext_vector_type(2)));
typedef __bf16 bf16x8 __attribute__((ext_vector_type(8)));
typedef _Float16 f16x2 __attribute__((ext_vector_type(2)));
typedef _Float16 f16x8 __attribute__((ext_vector_type(8)));
typedef __amdgpu_buffer_rsrc_t rsrc_t;
#define GAS __attribute__((address_space(1)))
#define LAS __attribute__((address_space(3)))
#define DEV __device__ __forceinline__

constexpr int L = 2048, D = 512, NM = 128, F = 1792;
constexpr int WS_EPOCH = 0;
constexpr int FSTR = 64;
constexpr int WS_FH = 4096;
constexpr int WS_FX = WS_FH + 256 * FSTR;
constexpr int WS_FS = WS_FX + 256 * FSTR;
constexpr int WS_GS = WS_FS + 256 * FSTR;
constexpr int WS_GC = WS_GS + L * 8;
constexpr int WS_XT = 1 << 17;
constexpr int WS_ST = WS_XT + (2 << 20);
constexpr int WS_HF = WS_ST + (2 << 20);
constexpr int WS_BYTES = WS_HF + L * F * 2;
#define L_PRE 2
#define OUT_AUX 16
#define LOADER_PRIO 0
#define LOADER_DELAY 5
constexpr int SC1 = 16;
#define XB 0
#define FXS 16
#define FXP 0
#define W2_A 4
#define W2_B 14
#define HROT 7
#define EXP_PRIO 2
#define MAIN_PRE_T 4
#define W2_PRE 14
#define W2_EARLY 12
constexpr int CODE_BYTES = 36 * 1024;
constexpr unsigned SPIN_CAP = 1u << 16;
constexpr int G1_SLOT = 32768, G1_R = 4;
constexpr int KG_ROW = 272;
constexpr int KG_WAVE = 53 * KG_ROW;
constexpr int KG_TAB = 3 * G1_SLOT;
constexpr int KG_K = KG_TAB + 4 * KG_WAVE;
constexpr int KG_KCH = 676 * 4;
constexpr int SY = KG_K + 2 * KG_KCH;
constexpr int GR = SY + 128;
constexpr int LDS_USED = GR + 1024;
constexpr int CV_KS = 4288;
constexpr int CV_K = 0;
constexpr int CV_X = 8 * CV_KS;
constexpr int CV_XS = (256 + 2048 + 16) * 2;
constexpr int CV_E = CV_X + 2 * CV_XS;
constexpr int G2_S = 4 * G1_SLOT;
constexpr int LDS_BYTES = 160 * 1024;
static_assert(WS_GC + F * 8 <= WS_XT && G2_S + 64 * 144 <= KG_K && LDS_USED <= LDS_BYTES && CV_E % 16 == 0 && CV_E + 16384 <= 2 * G1_SLOT, "maps");

DEV float bf2f(unsigned v) { return __uint_as_float(v << 16); }
DEV float bflo(unsigned v) { return __uint_as_float(v << 16); }
DEV float bfhi(unsigned v) { return __uint_as_float(v & 0xffff0000u); }
DEV unsigned pk_bf16(float a, float b) { f32x2 f = {a, b}; return __builtin_bit_cast(unsigned, __builtin_convertvector(f, bf16x2)); }
DEV unsigned pk_f16(float a, float b) { f32x2 f = {a, b}; return __builtin_bit_cast(unsigned, __builtin_convertvector(f, f16x2)); }
DEV float rbf(float a) { return bflo(pk_bf16(a, a) & 0xffffu); }
DEV float dot2bf(unsigned a, unsigned b, float c) {
  return __builtin_amdgcn_fdot2_f32_bf16(__builtin_bit_cast(bf16x2, a), __builtin_bit_cast(bf16x2, b), c, false);
}
DEV float silu_f(float v) { return v * __builtin_amdgcn_rcpf(1.f + __builtin_amdgcn_exp2f(v * -1.44269504088896f)); }
DEV void lds_barrier() { asm volatile("s_waitcnt lgkmcnt(0)" ::: "memory"); __builtin_amdgcn_s_barrier(); asm volatile("" ::: "memory"); }
DEV void drain() { asm volatile("s_waitcnt vmcnt(0)" ::: "memory"); }
template <int CTRL> DEV float dpp_add(float v) {
  return v + __builtin_bit_cast(float, __builtin_amdgcn_update_dpp(0, __builtin_bit_cast(int, v), CTRL, 0xf, 0xf, true));
}
DEV float row16_sum(float v) { v = dpp_add<0xb1>(v); v = dpp_add<0x4e>(v); v = dpp_add<0x124>(v); return dpp_add<0x128>(v); }
DEV float swap32_add(float a, float b) {
  auto r = __builtin_amdgcn_permlane32_swap(__float_as_uint(a), __float_as_uint(b), false, false);
  return __uint_as_float(r[0]) + __uint_as_float(r[1]);
}
DEV float swap16_add(float a, float b) {
  auto r = __builtin_amdgcn_permlane16_swap(__float_as_uint(a), __float_as_uint(b), false, false);
  return __uint_as_float(r[0]) + __uint_as_float(r[1]);
}
DEV float rdlane(float v, int l) { return __uint_as_float(__builtin_amdgcn_readlane(__float_as_uint(v), l)); }
template <int I> struct IC { static constexpr int v = I; };
template <int B, int E, class Fn> DEV void static_for(Fn&& f) { if constexpr (B < E) { f(IC<B>{}); static_for<B + 1, E>(f); } }

struct Ts {};
#define STAMP(p, i)

struct Params {
  const u16 *x, *lar, *aim, *cre, *cim, *ldt, *dsk, *w1, *w2;
  u16* out;
  char* ws;
};

DEV void wait_flags(rsrc_t rs, int off, int n, v2u tag, int lane) {
#pragma unroll 1
  for (unsigned spins = 0; spins < SPIN_CAP; ++spins) {
    asm volatile("" ::: "memory");
    const v2u v = __builtin_amdgcn_raw_buffer_load_b64(rs, off + min(lane, n - 1) * FSTR, 0, SC1);
    if (__all(v.x == tag.x && v.y == tag.y)) break;
    __builtin_amdgcn_s_sleep(4);
  }
  __builtin_amdgcn_fence(__ATOMIC_ACQUIRE, "wavefront");
}
DEV void publish(rsrc_t rs, LAS char* sm, int word, unsigned nwaves, int off, v2u tag, int lane) {
  drain();
  if (lane == 0) {
    const unsigned old = __hip_atomic_fetch_add((LAS unsigned*)(sm + SY) + word, 1u, __ATOMIC_RELAXED, __HIP_MEMORY_SCOPE_WORKGROUP);
    if (old == nwaves - 1) __builtin_amdgcn_raw_buffer_store_b64(tag, rs, off, 0, SC1);
  }
}
DEV void wave_sync(LAS char* sm, int word, unsigned n) {
  asm volatile("s_waitcnt lgkmcnt(0)" ::: "memory");
  volatile LAS unsigned* c = (volatile LAS unsigned*)(sm + SY) + word;
  if ((threadIdx.x & 63) == 0) __hip_atomic_fetch_add((LAS unsigned*)c, 1u, __ATOMIC_RELAXED, __HIP_MEMORY_SCOPE_WORKGROUP);
#pragma unroll 1
  for (unsigned spins = 0; spins < 16 * SPIN_CAP && __builtin_amdgcn_readfirstlane(*c) != n; ++spins) {}
  asm volatile("" ::: "memory");
}

DEV void lds_set(LAS char* sm, int word) { *((volatile LAS unsigned*)(sm + SY) + word) = 1u; }
DEV void lds_wait(LAS char* sm, int word, unsigned want = 1u) {
  volatile LAS unsigned* c = (volatile LAS unsigned*)(sm + SY) + word;
#pragma unroll 1
  for (unsigned spins = 0; spins < 16 * SPIN_CAP && __builtin_amdgcn_readfirstlane(*c) != want; ++spins) __builtin_amdgcn_s_sleep(1);
  __builtin_amdgcn_fence(__ATOMIC_ACQUIRE, "wavefront");
  asm volatile("" ::: "memory");
}
DEV void bar() { __builtin_amdgcn_sched_barrier(0); asm volatile("" ::: "memory"); __builtin_amdgcn_s_barrier(); asm volatile("" ::: "memory"); __builtin_amdgcn_sched_barrier(0); }

DEV void ln_wave(const Params& p, Ts& ts, rsrc_t rs, int t8, int lane, v2u tag, const v4u (&xr)[8], v4u (&o)[8]) {
  float s[8], q[8], t[4], rz[2], mz[2];
#pragma unroll
  for (int r = 0; r < 8; ++r) {
    s[r] = 0.f; q[r] = 0.f;
#pragma unroll
    for (int j = 0; j < 4; ++j) { s[r] = dot2bf(xr[r][j], 0x3f803f80u, s[r]); q[r] = dot2bf(xr[r][j], xr[r][j], q[r]); }
  }
#pragma unroll
  for (int j = 0; j < 4; ++j) { t[j] = swap32_add(s[j], s[j + 4]); q[j] = swap32_add(q[j], q[j + 4]); }
#pragma unroll
  for (int u = 0; u < 2; ++u) {
    const float mean = row16_sum(swap16_add(t[u], t[u + 2])) * (1.f / D);
    const float var = rbf(fmaxf(row16_sum(swap16_add(q[u], q[u + 2])) * (1.f / D) - mean * mean, 0.f));
    const unsigned ms = pk_bf16(mean, __builtin_amdgcn_sqrtf(rbf(var + 1e-5f)));
    if ((lane & 15) == 0) __builtin_amdgcn_raw_buffer_store_b64((v2u){ms, tag.x}, rs, WS_GS + (t8 * 8 + u + 2 * (lane >> 4)) * 8, 0, SC1);
    rz[u] = __builtin_amdgcn_rcpf(bfhi(ms));
    mz[u] = -bflo(ms) * rz[u];
  }
  float rr[8], mm[8];
#pragma unroll
  for (int r = 0; r < 8; ++r) { rr[r] = rdlane(rz[r & 1], 16 * (r >> 1)); mm[r] = rdlane(mz[r & 1], 16 * (r >> 1)); }
  STAMP(p, 21);
#pragma unroll
  for (int j = 0; j < 4; ++j) {
    f32x2 v[8];
#pragma unroll
    for (int r = 0; r < 8; ++r) v[r] = __builtin_elementwise_fma((f32x2){bflo(xr[r][j]), bfhi(xr[r][j])}, (f32x2){rr[r], rr[r]}, (f32x2){mm[r], mm[r]});
#pragma unroll
    for (int e = 0; e < 2; ++e) {
#pragma unroll
      for (int i = 0; i < 4; ++i) o[2 * j + e][i] = pk_f16(v[2 * i][e], v[2 * i + 1][e]);
      asm volatile("" : "+v"(o[2 * j + e]));
    }
  }
  STAMP(p, 27);
}

DEV void cs_part(const Params& p, LAS char* sm, int cb, int hw, int lane) {
  v4u wr[32];
#pragma unroll
  for (int i = 0; i < 32; ++i) wr[i] = *(const GAS v4u*)((const GAS char*)p.w1 + (size_t)(hw * 256 + i * 8 + (lane >> 3)) * (F * 2) + cb * 128 + (lane & 7) * 16);
  __builtin_amdgcn_sched_barrier(0);
  unsigned sel_lo = 0x00003f80u, sel_hi = 0x3f800000u;
  asm volatile("" : "+s"(sel_lo), "+s"(sel_hi));
  float c[8] = {0.f, 0.f, 0.f, 0.f, 0.f, 0.f, 0.f, 0.f}, t[4], y[2];
#pragma unroll
  for (int i = 0; i < 32; ++i)
#pragma unroll
    for (int j = 0; j < 4; ++j) { c[2 * j] = dot2bf(sel_lo, wr[i][j], c[2 * j]); c[2 * j + 1] = dot2bf(sel_hi, wr[i][j], c[2 * j + 1]); }
#pragma unroll
  for (int j = 0; j < 4; ++j) t[j] = swap32_add(c[j], c[j + 4]);
#pragma unroll
  for (int u = 0; u < 2; ++u) y[u] = dpp_add<0x128>(swap16_add(t[u], t[u + 2]));
  if ((lane & 8) == 0) *(LAS f32x2*)(sm + 65536 + (hw * 64 + (lane & 7) * 8 + 2 * (lane >> 4)) * 4) = (f32x2){y[0], y[1]};
}

struct KPar { unsigned ldt, lar, aim, c0, c1, c2, c3, dsk; };
DEV int chan_of(int b, int ch) { return 16 * (b >> 3) + 8 * ch + (b & 7); }
DEV f32x2 crot(f32x2 w) { return (f32x2){-w.y, w.x}; }
DEV f32x2 cmul(f32x2 a, f32x2 w, f32x2 wr) { return __builtin_elementwise_fma(a.yy, wr, a.xx * w); }
DEV f32x2 cexp_at(float a2, float rv, float pos) {
  const float e = __builtin_amdgcn_exp2f(a2 * pos);
  const float ph = __builtin_amdgcn_fractf(rv * pos);
  return (f32x2){__builtin_amdgcn_cosf(ph), __builtin_amdgcn_sinf(ph)} * e;
}
DEV float exp_acc(float x) {
  const float t = x * 1.44269504088896f;
  const float r = fmaf(x, 1.92596299112661e-8f, fmaf(x, 1.44269504088896f, -t));
  const float e = __builtin_amdgcn_exp2f(t);
  return fmaf(e, r * 0.693147180559945f, e);
}
DEV void kgen(const Params& p, Ts& ts, LAS char* sm, const KPar& k, int w, int lane) {
  const int ch = w >> 1;
  const float dt = rbf(exp_acc(bf2f(k.ldt)));
  const float a2 = -rbf(exp_acc(bf2f(k.lar))) * dt * 1.44269504088896f;
  const float rv = bf2f(k.aim) * dt * 0.159154943091895f;
  LAS char* t = sm + KG_TAB + w * KG_WAVE + lane * 4;
  auto put = [&](int row, f32x2 u) { *(LAS unsigned*)(t + row * KG_ROW) = pk_f16(u.x, u.y); };
  {
    const f32x2 w8 = cexp_at(a2, -rv, 8.f), w8r = crot(w8);
    const f32x2 w64 = cexp_at(a2, -rv, 64.f), w64r = crot(w64), w128 = cmul(w64, w64, w64r);
    f32x2 v[4] = {{1.f, 0.f}, w64, w128, cmul(w128, w64, w64r)};
#pragma unroll
    for (int i = 0; i < 8; ++i)
#pragma unroll
      for (int c = 0; c < 4; ++c) { put(21 + 8 * c + i, v[c]); if (i < 7) v[c] = cmul(v[c], w8, w8r); }
  }
  {
    unsigned mx = max(max(k.c0 & 0x7fffu, k.c1 & 0x7fffu), max(k.c2 & 0x7fffu, k.c3 & 0x7fffu));
    mx = max(mx, (unsigned)__builtin_amdgcn_update_dpp(0, (int)mx, 0xb1, 0xf, 0xf, true));
    mx = max(mx, (unsigned)__builtin_amdgcn_update_dpp(0, (int)mx, 0x4e, 0xf, 0xf, true));
    mx = max(mx, (unsigned)__builtin_amdgcn_update_dpp(0, (int)mx, 0x124, 0xf, 0xf, true));
    mx = max(mx, (unsigned)__builtin_amdgcn_update_dpp(0, (int)mx, 0x128, 0xf, 0xf, true));
    unsigned ex = max(max(__builtin_amdgcn_readlane(mx, 0), __builtin_amdgcn_readlane(mx, 16)), max(__builtin_amdgcn_readlane(mx, 32), __builtin_amdgcn_readlane(mx, 48))) >> 7;
    ex = min(max(ex, 1u), 253u);
    const float sc = __uint_as_float((254u - ex) << 23), isc = __uint_as_float(ex << 23);
    if (lane == 0) *(LAS f32x2*)(sm + SY + 64 + ch * 8) = (f32x2){2.f * dt * isc, bf2f(k.dsk)};
    const f32x2 c = (f32x2){bf2f(k.c0), bf2f(k.c1)} * sc;
    const f32x2 w1 = cexp_at(a2, rv, 1.f), e256 = cexp_at(a2, rv, 256.f), e256r = crot(e256), e512 = cmul(e256, e256, e256r), e1024 = cmul(e512, e512, crot(e512));
    const f32x2 w1r = crot(w1), w2 = cmul(w1, w1, w1r), w2r = crot(w2), w4 = cmul(w2, w2, w2r), w4r = crot(w4), cr = crot(c);
    f32x2 u = c, u4 = cmul(c, w4, w4r), u2 = cmul(e256, c, cr);
#pragma unroll
    for (int i = 0; i < 4; ++i) {
      put(i, u); put(4 + i, u4); put(8 + i, u2);
      if (i < 3) { u = cmul(u, w1, w1r); u4 = cmul(u4, w1, w1r); u2 = cmul(u2, w2, w2r); }
    }
    const f32x2 u512 = cmul(e512, c, cr), u768 = cmul(u512, e256, e256r), u1024 = cmul(e1024, c, cr), u1024r = crot(u1024);
    const f32x2 u1536 = cmul(e512, u1024, u1024r);
    put(12, u512); put(13, cmul(u512, w4, w4r)); put(14, u768); put(15, cmul(u768, w4, w4r));
    put(16, u1024); put(17, cmul(u1024, e256, e256r)); put(18, u1536); put(19, cmul(u1536, e256, e256r)); put(20, cmul(e1024, u1024, u1024r));
  }
  asm volatile("" ::: "memory");
  STAMP(p, 17);
  {
    const int h = lane >> 5, b = lane & 31;
    const LAS char* ua = sm + KG_TAB + w * KG_WAVE + b * KG_ROW + h * 16;
    f32x16 acc = {};
    f16x8 fa[8], fb[8];
#pragma unroll
    for (int s = 0; s < 8; ++s) { fa[s] = *(const LAS f16x8*)(ua + s * 32); fb[s] = *(const LAS f16x8*)(ua + 21 * KG_ROW + s * 32); }
    __builtin_amdgcn_sched_barrier(0);
#pragma unroll
    for (int s = 0; s < 8; ++s) acc = __builtin_amdgcn_mfma_f32_32x32x16_f16(fa[s], fb[s], acc, 0, 0, 0);
    STAMP(p, 18);
    LAS _Float16* kt = (LAS _Float16*)(sm + KG_K + ch * KG_KCH) + 8 + (w & 1);
    auto add = [&](int i, float v) { kt[2 * i] = (_Float16)v; };
    const int i0 = 4 * h + 8 * b, i1 = h ? 384 + 2 * b : 256 + 4 * b, i1s = h ? 64 : 2;
#pragma unroll
    for (int r = 0; r < 4; ++r) add(i0 + r, acc[r]);
    add(i1, acc[4]); add(i1 + 1, acc[5]); add(i1 + i1s, acc[6]); add(i1 + i1s + 1, acc[7]);
    add(512 + 128 * h + b, acc[8]);
    if (h == 0) { add(544 + b, acc[9]); add(576 + b, acc[10]); add(608 + b, acc[11]); }
  }
}
struct KIdx { unsigned a[11]; };
DEV KIdx kexpand_idx(int hw, int lane) {
  KIdx ix;
  const int pp = (hw & 1) * 64 + lane;
#pragma unroll
  for (int i = 0; i < 11; ++i) {
    const int lag = 8 * pp - 3 + i;
    const int code = (int)(pk_bf16((float)lag, 0.f) & 0xffffu) - 0x4280;
    ix.a[i] = KG_K + (hw >> 1) * KG_KCH + 16 + 4 * (lag < 128 ? lag : code);
    asm volatile("" : "+v"(ix.a[i]));
  }
  return ix;
}
DEV void kexpand(LAS char* sm, int hw, int lane, const KIdx& ix) {
  const int ch = hw >> 1;
  const LAS unsigned* kt = (const LAS unsigned*)(sm + KG_K + ch * KG_KCH) + 4;
  LAS char* kc = sm + CV_K + ch * 4 * CV_KS;
  auto sum2 = [&](unsigned hi, unsigned lo) {
    const unsigned a = __builtin_amdgcn_perm(hi, lo, 0x05040100u), b = __builtin_amdgcn_perm(hi, lo, 0x07060302u);
    return __builtin_bit_cast(unsigned, __builtin_bit_cast(f16x2, a) + __builtin_bit_cast(f16x2, b));
  };
  auto emit = [&](int pp, const unsigned (&pr)[10]) {
#pragma unroll
    for (int sg = 0; sg < 4; ++sg) *(LAS v4u*)(kc + sg * CV_KS + (516 - 2 * pp) * 8) = (v4u){pr[6 + sg], pr[4 + sg], pr[2 + sg], pr[sg]};
  };
  const int p3 = 128 + (hw & 1) * 64 + lane;
  const unsigned ka = kt[384 + p3], kb = kt[385 + p3], k0 = kt[p3 == 128 ? 511 : 384 + p3];
  {
    const int pp = (hw & 1) * 64 + lane;
    unsigned kv[12], pr[10], ev[6];
#pragma unroll
    for (int i = 0; i < 11; ++i) kv[i] = *(const LAS unsigned*)(sm + ix.a[i]);
    kv[11] = kv[10];
#pragma unroll
    for (int i = 0; i < 6; ++i) ev[i] = sum2(kv[2 * i], kv[2 * i + 1]);
#pragma unroll
    for (int i = 0; i < 5; ++i) { pr[2 * i] = ev[i]; pr[2 * i + 1] = __builtin_amdgcn_alignbit(ev[i], ev[i + 1], 16); }
    emit(pp, pr);
  }
  {
    const int pp = p3;
    const unsigned ab = sum2(ka, kb);
    const unsigned a0 = sum2(k0, ka);
    const unsigned aa = __builtin_amdgcn_perm(ab, ab, 0x03020302u), bb = __builtin_amdgcn_perm(ab, ab, 0x01000100u);
    const unsigned pr[10] = {a0, aa, aa, aa, aa, aa, (pp & 1) ? ab : aa, (pp & 1) ? bb : ab, bb, bb};
    emit(pp, pr);
  }
  if (lane < 20) *(LAS v4u*)(lane < 4 ? kc + ((hw & 1) * 2 + (lane >> 1)) * CV_KS + (518 + 2 * (lane & 1)) * 8 : sm + CV_X + ch * CV_XS + ((hw & 1) * 16 + lane - 4) * 16) = (v4u){0u, 0u, 0u, 0u};
}

DEV int ks_of(int w, int rl, int i) { const int k = i + (rl >> 3); return 7 * ((w + rl) & 7) + (k >= 7 ? k - 7 : k); }
template <int LO, int HI> DEV void w2_load(rsrc_t rw, int woff, int w, int rl, v4u (&wr)[28]) {
#pragma unroll
  for (int i = LO; i < HI; ++i) wr[i] = __builtin_amdgcn_raw_buffer_load_b128(rw, woff + (i & 3) * 1024, ks_of(w, rl, i >> 2) * 32768, 0);
}
DEV void w2_transpose(v4u* r) {
  unsigned P[8], Q[8];
  asm("s_mov_b64 vcc, %32\n\ts_nop 0\n\t"
#define CN(d, a, b) "v_cndmask_b32_dpp %" #d ", %" #a ", %" #b ", vcc row_ror:8 row_mask:0xf bank_mask:0xf\n\t"
      CN(0, 18, 16) CN(1, 19, 17) CN(2, 22, 20) CN(3, 23, 21) CN(4, 26, 24) CN(5, 27, 25) CN(6, 30, 28) CN(7, 31, 29)
      "s_not_b64 vcc, vcc\n\t"
      CN(8, 16, 18) CN(9, 17, 19) CN(10, 20, 22) CN(11, 21, 23) CN(12, 24, 26) CN(13, 25, 27) CN(14, 28, 30) CN(15, 29, 31)
#undef CN
      : "=&v"(P[0]), "=&v"(P[1]), "=&v"(P[2]), "=&v"(P[3]), "=&v"(P[4]), "=&v"(P[5]), "=&v"(P[6]), "=&v"(P[7]),
        "=&v"(Q[0]), "=&v"(Q[1]), "=&v"(Q[2]), "=&v"(Q[3]), "=&v"(Q[4]), "=&v"(Q[5]), "=&v"(Q[6]), "=&v"(Q[7])
      : "v"(r[0][0]), "v"(r[0][1]), "v"(r[0][2]), "v"(r[0][3]), "v"(r[1][0]), "v"(r[1][1]), "v"(r[1][2]), "v"(r[1][3]),
        "v"(r[2][0]), "v"(r[2][1]), "v"(r[2][2]), "v"(r[2][3]), "v"(r[3][0]), "v"(r[3][1]), "v"(r[3][2]), "v"(r[3][3]), "s"(0x00ff00ff00ff00ffull)
      : "vcc", "scc");
#pragma unroll
  for (int t = 0; t < 4; ++t) {
    const unsigned sel = (t & 1) ? 0x07060302u : 0x05040100u;
    const int u = t >> 1;
    r[t] = (v4u){__builtin_amdgcn_perm(P[2 + u], P[u], sel), __builtin_amdgcn_perm(P[6 + u], P[4 + u], sel),
                 __builtin_amdgcn_perm(Q[2 + u], Q[u], sel), __builtin_amdgcn_perm(Q[6 + u], Q[4 + u], sel)};
  }
  asm volatile("" : "+v"(r[0]), "+v"(r[1]), "+v"(r[2]), "+v"(r[3]));
}

struct Loader { const char* gb; int cstep, gstep, rot; unsigned slot, v[4]; };
DEV Loader loader_init(const Params& p, LAS char* sm, int rt, int ct, int rot, int hw, int lane) {
  Loader ld;
  const u64 m = -(u64)hw;
  ld.gb = (const char*)((((u64)p.w1 + ct * 256) & m) | (((u64)p.x + rt * 128 * 1024) & ~m));
  ld.cstep = 128 + hw * (64 * F * 2 - 128);
  ld.gstep = 32768 + hw * (16 * F * 2 - 32768);
  ld.rot = rot;
  ld.slot = (unsigned)(size_t)sm + hw * 16384;
  const unsigned base = (lane >> (3 + hw)) * (1024 + hw * (F * 2 - 1024)) + (((lane & (7 + 8 * hw)) ^ ((lane >> 4) << (2 * hw))) << 4);
#pragma unroll
  for (int i = 0; i < 4; ++i) ld.v[i] = (base ^ (((i & 1) << 6) + hw * (((i & 2) << 4) - ((i & 1) << 6)))) + i * (ld.gstep >> 2);
  return ld;
}
DEV void loader_issue(const Loader& ld, int c) {
  const char* g0 = ld.gb + (size_t)(((c + ld.rot) & 7) * ld.cstep);
  const char *g1 = g0 + ld.gstep, *g2 = g1 + ld.gstep, *g3 = g2 + ld.gstep;
#define GL4(S) "global_load_lds_dwordx4 %0, " S "\n\ts_add_u32 m0, m0, 0x400\n\ts_nop 0\n\tglobal_load_lds_dwordx4 %1, " S "\n\ts_add_u32 m0, m0, 0x400\n\ts_nop 0\n\t" \
               "global_load_lds_dwordx4 %2, " S "\n\ts_add_u32 m0, m0, 0x400\n\ts_nop 0\n\tglobal_load_lds_dwordx4 %3, " S "\n\ts_add_u32 m0, m0, 0x400\n\ts_nop 0\n\t"
  asm volatile("s_mov_b32 m0, %8\n\ts_nop 0\n\t" GL4("%4") GL4("%5") GL4("%6") GL4("%7")
               :: "v"(ld.v[0]), "v"(ld.v[1]), "v"(ld.v[2]), "v"(ld.v[3]), "s"(g0), "s"(g1), "s"(g2), "s"(g3), "s"(ld.slot + (c & 3) * G1_SLOT) : "memory", "scc");
#undef GL4
}
struct G1Frag { v4u a[4], b[4]; };
DEV void g1_main(const Params& p, Ts& ts, LAS char* sm, rsrc_t rs, int rt, int ct, int w, int lane, v2u tag) {
  const int wf = w & 1, wt = w >> 1, g = lane >> 4, r16 = lane & 15;
  f32x4 acc[4][4] = {};
  const int q4 = r16 >> 2, p4 = lane & 3;
  int aoff[2], boff[2];
#pragma unroll
  for (int i = 0; i < 2; ++i) aoff[i] = 16384 + (8 * g + q4) * 256 + (((8 * wf + 4 * i + (p4 >> 1)) ^ (q4 << 2) ^ ((g & 1) << 1)) << 4) + 8 * (p4 & 1);
  const int tok0 = wt * 64 + r16;
#pragma unroll
  for (int s = 0; s < 2; ++s) boff[s] = tok0 * 128 + (((4 * s + g) ^ ((tok0 >> 1) & 7)) << 4);
  auto rd = [&](int c, int s) {
    G1Frag f;
    LAS char* base = sm + (c % G1_R) * G1_SLOT;
#pragma unroll
    for (int i = 0; i < 4; ++i) {
      const int o = (aoff[i >> 1] ^ ((i & 1) << 5)) + s * 8192;
      const v2u a0 = __builtin_bit_cast(v2u, __builtin_amdgcn_ds_read_tr16_b64_v4i16((LAS s16x4*)(base + o)));
      const v2u a1 = __builtin_bit_cast(v2u, __builtin_amdgcn_ds_read_tr16_b64_v4i16((LAS s16x4*)(base + o + 1024)));
      f.a[i] = (v4u){a0.x, a0.y, a1.x, a1.y};
      f.b[i] = *(const LAS v4u*)(base + boff[s] + 2048 * i);
    }
    return f;
  };
  G1Frag fr[16];
#pragma unroll
  for (int st = -1; st < 16; ++st) {
    const int ld = st + 1;
    if (ld < 16) {
      if ((ld & 1) == 0 && ld < 14) lds_barrier();
      if (ld == 0) STAMP(p, 19);
      if (ld == 2) STAMP(p, 20);
      if (ld == 4) STAMP(p, 24);
      if (ld == 8) STAMP(p, 25);
      if (ld == 12) STAMP(p, 26);
      fr[ld] = rd(ld >> 1, ld & 1);
    }
    if (st >= 0) {
#pragma unroll
      for (int i = 0; i < 4; ++i)
#pragma unroll
        for (int j = 0; j < 4; ++j)
          acc[i][j] = __builtin_amdgcn_mfma_f32_16x16x32_bf16(__builtin_bit_cast(bf16x8, fr[st].a[i]), __builtin_bit_cast(bf16x8, fr[st].b[j]), acc[i][j], 0, 0, 0);
#pragma unroll
      for (int i = 0; i < 12; ++i) {
        __builtin_amdgcn_sched_group_barrier(0x8, 1, 0);
        __builtin_amdgcn_sched_group_barrier(0x100, 1, 0);
      }
      __builtin_amdgcn_sched_group_barrier(0x8, 4, 0);
    }
    __builtin_amdgcn_sched_barrier(0);
  }
  STAMP(p, 3);
  __builtin_amdgcn_s_setprio(1);
  lds_wait(sm, 8);
  unsigned gs[4], gc[4];
#pragma unroll
  for (int i = 0; i < 4; ++i) {
    gs[i] = *(const LAS unsigned*)(sm + GR + (tok0 + 16 * i) * 4);
    gc[i] = *(const LAS unsigned*)(sm + GR + 512 + (wf * 64 + r16 + 16 * i) * 4);
  }
  STAMP(p, 30);
  float c1[4], sd[4];
#pragma unroll
  for (int j = 0; j < 4; ++j) {
    const unsigned m2 = __builtin_amdgcn_perm(gs[j], gs[j], 0x01000100u) ^ 0x80008000u;
    const v2u bm = {g == 0 ? m2 : 0u, 0u};
#pragma unroll
    for (int i = 0; i < 4; ++i)
      acc[i][j] = __builtin_amdgcn_mfma_f32_16x16x16bf16_1k(__builtin_bit_cast(s16x4, (v2u){gc[i], 0u}), __builtin_bit_cast(s16x4, bm), acc[i][j], 0, 0, 0);
    sd[j] = bfhi(gs[j]);
    c1[j] = __builtin_amdgcn_rcpf(sd[j]) * -1.44269504088896f;
  }
  const int hbase = WS_HF + ((2 * rt + wt) * 56 + 4 * ct + 2 * wf) * 4096 + lane * 16;
#pragma unroll
  for (int m = 0; m < 2; ++m)
#pragma unroll
    for (int j = 0; j < 4; ++j) {
      v4u o;
#pragma unroll
      for (int e = 0; e < 4; ++e) {
        const f32x4 a4 = acc[2 * m + (e >> 1)][j];
        const f32x2 t = (e & 1) ? a4.zw : a4.xy;
        const f32x2 u = t * c1[j];
        const f32x2 ex = {__builtin_amdgcn_exp2f(u.x), __builtin_amdgcn_exp2f(u.y)};
        const f32x2 dn = __builtin_elementwise_fma(ex, (f32x2){sd[j], sd[j]}, (f32x2){sd[j], sd[j]});
        const f32x2 r = t * (f32x2){__builtin_amdgcn_rcpf(dn.x), __builtin_amdgcn_rcpf(dn.y)};
        o[e] = pk_bf16(r.x, r.y);
      }
      __builtin_amdgcn_raw_buffer_store_b128(o, rs, hbase + m * 4096 + j * 1024, 0, SC1);
      __builtin_amdgcn_sched_barrier(0);
    }
  STAMP(p, 4);
  lds_set(sm, 12 + w);
  publish(rs, sm, 1, 4, WS_FH + (rt * 16 + ct) * FSTR, tag, lane);
  __builtin_amdgcn_s_setprio(0);
}

DEV void conv(const Params& p, Ts& ts, LAS char* sm, rsrc_t rs, int b, int hw, int lane, v2u tag, v4u (&wr)[28]) {
  const int ch = hw >> 1, eh = hw & 1, g = lane >> 4, r16 = lane & 15, d = chan_of(b, ch);
  LAS char* xs = sm + CV_X + ch * CV_XS;
  lds_wait(sm, 4, 4u);
  STAMP(p, 6);
  const LAS char* ka = sm + CV_K + (ch * 4 + (r16 & 3)) * CV_KS + (517 - (r16 >> 2) + 2 * g) * 8;
  const LAS char* xa = xs + 512 + r16 * 32 + g * 16;
  auto lda = [&](const LAS char* ptr) {
    const v2u lo = *(const volatile LAS v2u*)ptr, hi = *(const volatile LAS v2u*)(ptr + 8);
    return __builtin_bit_cast(f16x8, (v4u){lo.x, lo.y, hi.x, hi.y});
  };
  f32x4 acc[8];
#pragma unroll
  for (int a = 0; a < 8; ++a) acc[a] = (f32x4){0.f, 0.f, 0.f, 0.f};
  const LAS char* ke = ka - 256 * eh - 3584 - 256;
  const LAS char* xe = xa - 256 * eh - 256;
  f16x8 A[4][8], B[4][8];
  auto ld = [&](int e, int i) { if (e < 4) { A[e][i] = lda(ke + 3584 - 512 * i + 224 - 64 * e); B[e][i] = *(const LAS f16x8*)(xe + 224 - 64 * e + 512 * i); } };
  ld(0, 0); ld(0, 7); ld(0, 1); ld(0, 6); ld(0, 2); ld(0, 5); ld(0, 3); ld(0, 4);
  __builtin_amdgcn_sched_barrier(0);
  static_for<0, 4>([&](auto ee) {
    constexpr int e = decltype(ee)::v;
    static_for<0, 8>([&](auto ll) {
      constexpr int lv = 7 - decltype(ll)::v;
      constexpr int n = (7 - lv < lv ? 7 - lv : lv - 1) + 1;
      if constexpr (lv == 4) ld(e + 1, 0);
      if constexpr (lv == 3) ld(e + 1, 1);
      if constexpr (lv < 7 && lv >= 1) ld(e + 1, lv + 1);
#pragma unroll
      for (int i = 0; i < n; ++i) {
        acc[lv + i] = __builtin_amdgcn_mfma_f32_16x16x32_f16(A[e][i], B[e][lv], acc[lv + i], 0, 0, 0);
        acc[lv + i] = __builtin_amdgcn_mfma_f32_16x16x32_f16(A[e][lv], B[e][i], acc[lv + i], 0, 0, 0);
      }
      if constexpr (2 * lv <= 7) acc[2 * lv] = __builtin_amdgcn_mfma_f32_16x16x32_f16(A[e][lv], B[e][lv], acc[2 * lv], 0, 0, 0);
#pragma unroll
      for (int i = 0; i < 6; ++i) { __builtin_amdgcn_sched_group_barrier(0x8, 1, 0); __builtin_amdgcn_sched_group_barrier(0x100, 1, 0); }
      __builtin_amdgcn_sched_barrier(0);
    });
  });
  __builtin_amdgcn_s_setprio(2);
  LAS f32x4* ex = (LAS f32x4*)(sm + CV_E);
  const f32x2 md = *(const LAS f32x2*)(sm + SY + 64 + ch * 8);
  const int t0 = 1024 * eh + 16 * r16 + 4 * g;
  v2u xr[4];
#pragma unroll
  for (int i = 0; i < 4; ++i) xr[i] = *(const LAS v2u*)(xs + 512 + (t0 + 256 * i) * 2);
  f32x4 own[4];
  if (eh) {
#pragma unroll
    for (int i = 0; i < 4; ++i) { ex[(hw * 4 + i) * 64 + lane] = acc[i]; own[i] = acc[4 + i]; }
    asm volatile("; tiles 4..7" ::: "memory");
  } else {
#pragma unroll
    for (int i = 0; i < 4; ++i) { ex[(hw * 4 + i) * 64 + lane] = acc[4 + i]; own[i] = acc[i]; }
    asm volatile("; tiles 0..3" ::: "memory");
  }
  asm volatile("s_waitcnt lgkmcnt(0)" ::: "memory");
  if (lane == 0) __hip_atomic_fetch_add((LAS unsigned*)(sm + SY) + 6 + ch, 1u, __ATOMIC_RELAXED, __HIP_MEMORY_SCOPE_WORKGROUP);
  const float mult = rdlane(md.x, 0), dsk = rdlane(md.y, 0);
#pragma unroll
  for (int i = 0; i < 4; ++i) {
    const unsigned xr0 = xr[i].x, xr1 = xr[i].y;
    const f16x2 x0 = __builtin_bit_cast(f16x2, xr0), x1 = __builtin_bit_cast(f16x2, xr1);
    own[i] = own[i] * mult + (f32x4){(float)x0.x, (float)x0.y, (float)x1.x, (float)x1.y} * dsk;
    asm volatile("" : "+v"(own[i]));
  }
  STAMP(p, 7);
  lds_wait(sm, 6 + ch, 2u);
#pragma unroll
  for (int i = 0; i < 4; ++i) {
    const f32x4 y = ex[((hw ^ 1) * 4 + i) * 64 + lane] * mult + own[i];
    __builtin_amdgcn_raw_buffer_store_b64((v2u){pk_bf16(y[0], y[1]), pk_bf16(y[2], y[3])}, rs, WS_ST + d * 4096 + (t0 + 256 * i) * 2, 0, SC1);
  }
  STAMP(p, 5);
  __builtin_amdgcn_sched_barrier(0);
  w2_transpose(wr + 12); w2_transpose(wr + 16); w2_transpose(wr + 20);
  __builtin_amdgcn_sched_barrier(0);
  publish(rs, sm, 2, 4, WS_FS + b * FSTR, tag, lane);
  __builtin_amdgcn_s_setprio(0);
}

template <bool MAIN> DEV void gemm2(const Params& p, Ts& ts, LAS char* sm, rsrc_t rs, int rt2, int ct2, int rl, bool isL, int w, int lane, v2u tag, v2u hf, v4u (&wr)[28]) {
  const int g = lane >> 4, hb = (lane >> 3) & 1, cb = lane & 7;
  STAMP(p, 31);
  {
    const int e8 = (w + rl) & 7;
    const unsigned need = (MAIN && w == 0) ? 0x3fffu : ((2u << ((7 * e8 + 6) >> 2)) - (1u << ((7 * e8) >> 2)));
    volatile LAS unsigned* c = (volatile LAS unsigned*)(sm + SY) + 5;
    if (MAIN && w == 0) {
#pragma unroll 1
      for (unsigned spins = 0; spins < SPIN_CAP; ++spins) {
        const unsigned m = (unsigned)__ballot(hf.x == tag.x && hf.y == tag.y) & 0x3fffu;
        *c = m;
        if (m == need) break;
        __builtin_amdgcn_s_sleep(2);
        asm volatile("" ::: "memory");
        hf = __builtin_amdgcn_raw_buffer_load_b64(rs, WS_FH + ((rt2 >> 1) * 16 + min(lane, 13)) * FSTR, 0, SC1);
      }
    } else {
#pragma unroll 1
      for (unsigned spins = 0; spins < 16 * SPIN_CAP && (__builtin_amdgcn_readfirstlane(*c) & need) != need; ++spins) __builtin_amdgcn_s_sleep(1);
    }
    __builtin_amdgcn_fence(__ATOMIC_ACQUIRE, "wavefront");
    asm volatile("" ::: "memory");
  }
  STAMP(p, 10);
  v4u ha[28];
  const int hoff = WS_HF + rt2 * 56 * 4096 + lane * 16;
  auto hload = [&](int i) { return __builtin_amdgcn_raw_buffer_load_b128(rs, hoff + (i & 3) * 1024, ks_of(w, rl, i >> 2) * 4096, SC1); };
#pragma unroll
  for (int i = 0; i < 16; ++i) ha[i] = hload(i);
  __builtin_amdgcn_sched_barrier(0);
#pragma unroll
  for (int ks = MAIN ? MAIN_PRE_T : 6; ks < 7; ++ks) { w2_transpose(wr + 4 * ks); __builtin_amdgcn_sched_barrier(0); }
  if (MAIN) STAMP(p, 16); else STAMP(p, 17);
  f32x4 acc[4][4] = {};
  auto mm = [&](int ks, int mt) {
#pragma unroll
    for (int t = 0; t < 4; ++t)
      acc[mt][t] = __builtin_amdgcn_mfma_f32_16x16x32_bf16(__builtin_bit_cast(bf16x8, ha[4 * ks + mt]), __builtin_bit_cast(bf16x8, wr[4 * ks + t]), acc[mt][t], 0, 0, 0);
  };
#pragma unroll
  for (int ks = 0; ks < 4; ++ks) {
#pragma unroll
    for (int mt = 0; mt < 4; ++mt) mm(ks, mt);
    __builtin_amdgcn_sched_barrier(0);
    if (ks == 0) { if (MAIN) STAMP(p, 21); else STAMP(p, 18); }
    if (ks == 3) { if (MAIN) STAMP(p, 22); else STAMP(p, 19); }
    if (ks < 2) {
#pragma unroll
      for (int i = 0; i < 6; ++i) { const int e = 6 * ks + i; ha[16 + 4 * (e % 3) + e / 3] = hload(16 + 4 * (e % 3) + e / 3); }
    }
    __builtin_amdgcn_sched_barrier(0);
  }
  lds_wait(sm, MAIN ? 1 : 2, MAIN && isL ? 0u : 4u);
  LAS f32x4* red = (LAS f32x4*)(sm + (w ^ 4) * 16384);
#pragma unroll
  for (int mt = 0; mt < 4; ++mt) {
#pragma unroll
    for (int ks = 4; ks < 7; ++ks) mm(ks, mt);
    if (mt > 0) {
#pragma unroll
      for (int t = 0; t < 4; ++t) red[((mt - 1) * 4 + t) * 64 + lane] = acc[mt - 1][t];
    }
#pragma unroll
    for (int i = 0; i < 4; ++i) { __builtin_amdgcn_sched_group_barrier(0x8, 3, 0); __builtin_amdgcn_sched_group_barrier(0x200, 1, 0); }
    __builtin_amdgcn_sched_barrier(0);
  }
#pragma unroll
  for (int t = 0; t < 4; ++t) red[(12 + t) * 64 + lane] = acc[3][t];
  STAMP(p, 11);
  const int tok = rt2 * 64 + 16 * w + 4 * g, chn = ct2 * 64 + 8 * cb + 4 * hb;
  v2u xv[4];
  f32x4 o[4], sf[4], xf[4];
  if (MAIN) {
#pragma unroll
    for (int i = 0; i < 4; ++i) xv[i] = *(const GAS v2u*)((const GAS char*)p.x + (size_t)(tok + i) * 1024 + chn * 2);
    if (w == 0) {
      lds_wait(sm, 2, 4u);
      wait_flags(rs, WS_FS + ct2 * 32 * FSTR, 32, tag, lane);
      lds_set(sm, 3);
    } else lds_wait(sm, 3);
#pragma unroll
    for (int i = 0; i < 2; ++i) {
      const int c = 16 * w + 8 * i + (lane >> 3);
      *(LAS v4u*)(sm + G2_S + c * 144 + (lane & 7) * 16) =
          __builtin_amdgcn_raw_buffer_load_b128(rs, WS_ST + (ct2 * 64 + c) * 4096 + rt2 * 128 + (lane & 7) * 16, 0, SC1);
    }
    wave_sync(sm, 10, 4);
#pragma unroll
    for (int t = 0; t < 4; ++t) {
      const LAS f32x4* r0 = (const LAS f32x4*)(sm + 65536) + (w * 4 + t) * 64 + lane;
      o[t] = (r0[0] + r0[1024]) + (r0[2048] + r0[3072]);
      const v2u sv = *(const LAS v2u*)(sm + G2_S + (8 * cb + 4 * hb + t) * 144 + 32 * w + 8 * g);
      sf[t] = (f32x4){bflo(sv.x), bfhi(sv.x), bflo(sv.y), bfhi(sv.y)};
      xf[t] = (f32x4){(t & 1) ? bfhi(xv[0][t >> 1]) : bflo(xv[0][t >> 1]), (t & 1) ? bfhi(xv[1][t >> 1]) : bflo(xv[1][t >> 1]),
                      (t & 1) ? bfhi(xv[2][t >> 1]) : bflo(xv[2][t >> 1]), (t & 1) ? bfhi(xv[3][t >> 1]) : bflo(xv[3][t >> 1])};
      asm volatile("" : "+v"(o[t]), "+v"(sf[t]), "+v"(xf[t]));
    }
  }
  STAMP(p, 28);
  lds_barrier();
  STAMP(p, 12);
  if (MAIN) {
    const rsrc_t ro = __builtin_amdgcn_make_buffer_rsrc(p.out, 0, L * D * 2, 0x00020000);
#pragma unroll
    for (int t = 0; t < 4; ++t) {
      const LAS f32x4* r0 = (const LAS f32x4*)sm + (w * 4 + t) * 64 + lane;
      o[t] += (r0[0] + r0[1024]) + (r0[2048] + r0[3072]);
      const f32x4 u = o[t] * -1.44269504088896f;
      const f32x4 dn = (f32x4){__builtin_amdgcn_exp2f(u[0]), __builtin_amdgcn_exp2f(u[1]), __builtin_amdgcn_exp2f(u[2]), __builtin_amdgcn_exp2f(u[3])} + 1.f;
      const f32x4 rc = {__builtin_amdgcn_rcpf(dn[0]), __builtin_amdgcn_rcpf(dn[1]), __builtin_amdgcn_rcpf(dn[2]), __builtin_amdgcn_rcpf(dn[3])};
      o[t] = __builtin_elementwise_fma(o[t] * rc, sf[t], xf[t]);
    }
#pragma unroll
    for (int i = 0; i < 4; ++i)
      __builtin_amdgcn_raw_buffer_store_b64((v2u){pk_bf16(o[0][i], o[1][i]), pk_bf16(o[2][i], o[3][i])}, ro, (tok + i) * 1024 + chn * 2, 0, OUT_AUX);
  }
  STAMP(p, 13);
}

__global__ void __launch_bounds__(512, 2) fused_kernel(Params p) {
  __shared__ __attribute__((aligned(16))) char smem[LDS_BYTES];
  LAS char* sm = (LAS char*)smem;
  const int tid = threadIdx.x, lane = tid & 63, w = __builtin_amdgcn_readfirstlane(tid >> 6);
  const int b = blockIdx.x, xcd = b & 7, idx = b >> 3;
  const bool isL = idx < 4;
  const int ii = idx - 4, rt = (xcd & 3) * 4 + (ii & 3), ct = (xcd >> 2) * 7 + (ii >> 2);
  const int rt2 = (xcd & 3) * 8 + (xcd >> 2) * 4 + (idx >> 3), ct2 = idx & 7;
  const int rl = ((2 * (idx >> 3) + 4 * (ct2 & 1)) & 7) + 8 * ((xcd + 3 * ((ct2 >> 1) & 1)) % 7);
  const int t8 = (xcd & 3) * 64 + (xcd >> 2) * 32 + idx;
  Ts ts = {};
  STAMP(p, 0);
  unsigned pf;
  asm volatile("global_load_dword %0, %1, %2" : "=v"(pf) : "v"(min((((idx * 8 + w) ^ ((lane & 3) << 7)) << 7), CODE_BYTES - 128)), "s"(__builtin_amdgcn_s_getpc() & ~(u64)255));
  __builtin_amdgcn_sched_barrier(0);
  const rsrc_t rs = __builtin_amdgcn_make_buffer_rsrc(p.ws, 0, WS_BYTES, 0x00020000);
  asm volatile("" ::"s"(p.x), "s"(p.lar), "s"(p.aim), "s"(p.cre), "s"(p.cim), "s"(p.ldt), "s"(p.dsk), "s"(p.w1), "s"(p.w2), "s"(p.ws));
  v2u ep;
  KPar kp;
  v4u early[8];
  asm volatile("global_load_dwordx2 %0, %1, %2 sc1" : "=v"(ep) : "v"(WS_EPOCH), "s"(p.ws));
  if (w < 4) {
    const unsigned d2 = chan_of(b, w >> 1) * 2, o = d2 * NM + ((w & 1) * 64 + lane) * 2, o2 = o ^ 128;
    asm volatile("global_load_ushort %0, %8, %11\n\tglobal_load_ushort %1, %9, %12\n\tglobal_load_ushort %2, %9, %13\n\t"
                 "global_load_ushort %3, %9, %14\n\tglobal_load_ushort %4, %9, %15\n\tglobal_load_ushort %5, %10, %14\n\t"
                 "global_load_ushort %6, %10, %15\n\tglobal_load_ushort %7, %8, %16"
                 : "=&v"(kp.ldt), "=&v"(kp.lar), "=&v"(kp.aim), "=&v"(kp.c0), "=&v"(kp.c1), "=&v"(kp.c2), "=&v"(kp.c3), "=&v"(kp.dsk)
                 : "v"(d2), "v"(o), "v"(o2), "s"(p.ldt), "s"(p.lar), "s"(p.aim), "s"(p.cre), "s"(p.cim), "s"(p.dsk));
  }
  if (w == 6) {
    const char* sb = (const char*)p.x + (size_t)t8 * 8192;
    const unsigned o = lane * 16, o2 = o + 4096;
    asm volatile("global_load_dwordx4 %0, %8, %10\n\tglobal_load_dwordx4 %1, %8, %10 offset:1024\n\tglobal_load_dwordx4 %2, %8, %10 offset:2048\n\t"
                 "global_load_dwordx4 %3, %8, %10 offset:3072\n\tglobal_load_dwordx4 %4, %9, %10\n\tglobal_load_dwordx4 %5, %9, %10 offset:1024\n\t"
                 "global_load_dwordx4 %6, %9, %10 offset:2048\n\tglobal_load_dwordx4 %7, %9, %10 offset:3072"
                 : "=&v"(early[0]), "=&v"(early[1]), "=&v"(early[2]), "=&v"(early[3]), "=&v"(early[4]), "=&v"(early[5]), "=&v"(early[6]), "=&v"(early[7])
                 : "v"(o), "v"(o2), "s"(sb));
  }
  KIdx kix;
  if (w >= 4) kix = kexpand_idx(w - 4, lane);
  Loader ld;
  if (__builtin_expect((w & 6) == 4 && !isL, 1)) {
    ld = loader_init(p, sm, rt, ct, 2 * (ii & 3) + 2 * (ii >> 2), w - 4, lane);
    __builtin_amdgcn_s_sleep(LOADER_DELAY);
#pragma unroll 1
    for (int c = 0; c < 3; ++c) loader_issue(ld, c);
    STAMP(p, 2);
  }
  __builtin_amdgcn_sched_barrier(0);
  auto make_tag = [&]() {
    unsigned lo, hi;
    asm volatile("v_readfirstlane_b32 %0, %2\n\tv_readfirstlane_b32 %1, %3" : "=s"(lo), "=s"(hi) : "v"(ep.x), "v"(ep.y), "v"(pf));
    const u64 t = (((u64)hi << 32) | lo) + 1ull;
    return (v2u){(unsigned)t, (unsigned)(t >> 32)};
  };
  if (__builtin_expect(w < 4, 1)) {
    __builtin_amdgcn_s_setprio(1);
    if (w == 0 && lane < 16) *(LAS unsigned*)(sm + SY + lane * 4) = 0u;
    if (w == 1 && lane < 8) *(LAS unsigned*)(sm + KG_K + (lane >> 2) * KG_KCH + (lane & 3) * 4) = 0u;
    __builtin_amdgcn_sched_barrier(0);
    drain();
    __builtin_amdgcn_sched_barrier(0);
    asm volatile("" : "+v"(kp.ldt), "+v"(kp.lar), "+v"(kp.aim), "+v"(kp.c0), "+v"(kp.c1), "+v"(kp.c2), "+v"(kp.c3), "+v"(kp.dsk));
    STAMP(p, 1);
    kgen(p, ts, sm, kp, w, lane);
    STAMP(p, 2);
  }
  asm volatile("; roles" ::: "memory");
  v4u xt[8];
  if (__builtin_expect(w == 6, 1)) {
    drain();
    __builtin_amdgcn_sched_barrier(0);
    STAMP(p, 1);
    asm volatile("" : "+v"(early[0]), "+v"(early[1]), "+v"(early[2]), "+v"(early[3]), "+v"(early[4]), "+v"(early[5]), "+v"(early[6]), "+v"(early[7]));
    ln_wave(p, ts, rs, t8, lane, make_tag(), early, xt);
  }
  asm volatile("; phases" ::: "memory");
  const rsrc_t rw = __builtin_amdgcn_make_buffer_rsrc((void*)p.w2, 0, F * D * 2, 0x00020000);
  const int woff = (16 * ((lane >> 3) & 1) + 4 * (lane >> 4)) * 1024 + ct2 * 128 + (lane & 7) * 16;
  auto finish = [&]() {
    __builtin_amdgcn_endpgm();
  };
  if (w < 4) {
    const v2u tag = make_tag();
    if (isL) {
      asm volatile("s_waitcnt lgkmcnt(0)" ::: "memory");
#pragma unroll 1
      for (int i = 0; i < 7; ++i) bar();
      __builtin_amdgcn_s_setprio(0);
      lds_wait(sm, 4, 4u);
    } else g1_main(p, ts, sm, rs, rt, ct, w, lane, tag);
    STAMP(p, 5);
    v4u wr[28];
    w2_load<0, 28>(rw, woff, w, rl, wr);
    v2u hf = {0u, 0u};
    if (w == 0) hf = __builtin_amdgcn_raw_buffer_load_b64(rs, WS_FH + ((rt2 >> 1) * 16 + min(lane, 13)) * FSTR, 0, SC1);
    __builtin_amdgcn_sched_barrier(0);
    STAMP(p, 23);
#pragma unroll
    for (int ks = 0; ks < MAIN_PRE_T; ++ks) {
      w2_transpose(wr + 4 * ks);
      __builtin_amdgcn_sched_barrier(0);
    }
    gemm2<true>(p, ts, sm, rs, rt2, ct2, rl, isL, w, lane, tag, hf, wr);
    finish();
  } else {
    v2u tag, f[4];
    v4u xv[8];
    int poff, pst1, pst2;
    if (w >= 6) {
      if (w == 7) drain();
      __builtin_amdgcn_sched_barrier(0);
      tag = make_tag();
      if (w == 6) {
        bar();
#pragma unroll
        for (int j = 0; j < 8; ++j) __builtin_amdgcn_raw_buffer_store_b128(xt[j], rs, WS_XT + t8 * 8192 + j * 1024 + lane * 16, 0, SC1);
        if (!isL) { bar(); bar(); }
        drain();
        if (lane == 0) __builtin_amdgcn_raw_buffer_store_b64(tag, rs, WS_FX + b * FXS, 0, SC1);
        STAMP(p, 22);
#pragma unroll 1
        for (int i = isL ? 1 : 3; i < 6; ++i) bar();
      } else {
#pragma unroll 1
        for (int i = 0; i < 6; ++i) bar();
      }
      poff = w == 7 ? WS_FX + lane * FXS : WS_GS + (isL ? 0 : rt * 1024) + lane * 8;
      pst1 = w == 7 ? 64 * FXS : 512;
      pst2 = w == 7 ? 128 * FXS : WS_GC - WS_GS + (isL ? 0 : (ct - rt) * 1024);
      if (!isL && (w == 6 || FXP == 0)) {
#pragma unroll
        for (int i = 0; i < 4; ++i) f[i] = __builtin_amdgcn_raw_buffer_load_b64(rs, poff, (i & 1) * pst1 + (i >> 1) * pst2, SC1);
      }
    } else if (isL) {
      const int cg = (xcd & 3) * 4 + idx;
      cs_part(p, sm, (xcd >> 2) * 14 + min(cg, 13), w - 4, lane);
      lds_barrier();
      STAMP(p, 16);
      if (w == 4 && cg < 14) {
        const float cs = *(const LAS float*)(sm + 65536 + lane * 4) + *(const LAS float*)(sm + 65536 + 256 + lane * 4);
        const float hi = rbf(cs);
        __builtin_amdgcn_raw_buffer_store_b64((v2u){pk_bf16(hi, cs - hi), make_tag().x}, rs, WS_GC + (((xcd >> 2) * 14 + cg) * 64 + lane) * 8, 0, SC1);
      }
#pragma unroll 1
      for (int i = 0; i < 5; ++i) bar();
    }
    __builtin_amdgcn_sched_barrier(0);
    v4u wr[28];
    w2_load<0, W2_A>(rw, woff, w, rl, wr);
    __builtin_amdgcn_sched_barrier(0);
    if (w < 6 && !isL) {
      STAMP(p, 3);
#pragma unroll 1
      for (int c = 2; c < 8; ++c) {
        if (c > 2) loader_issue(ld, c);
        if (c < 5) asm volatile("s_waitcnt vmcnt(%0)" ::"n"(32 + W2_A) : "memory");
        else asm volatile("s_waitcnt vmcnt(32)" ::: "memory");
        __builtin_amdgcn_s_barrier();
      }
    }
    __builtin_amdgcn_sched_barrier(0);
    w2_load<W2_A, W2_B>(rw, woff, w, rl, wr);
    __builtin_amdgcn_sched_barrier(0);
    if (w < 6) asm volatile("s_waitcnt vmcnt(%0)" ::"n"(W2_B - W2_A) : "memory");
    STAMP(p, 20);
    bar();
    auto poll4 = [&](unsigned tx, unsigned mx) {
#pragma unroll 1
      for (unsigned spins = 0; spins < SPIN_CAP; ++spins) {
        asm volatile("" ::: "memory");
#pragma unroll
        for (int i = 0; i < 4; ++i) f[i] = __builtin_amdgcn_raw_buffer_load_b64(rs, poff, (i & 1) * pst1 + (i >> 1) * pst2, SC1);
        unsigned bad = 0;
#pragma unroll
        for (int i = 0; i < 4; ++i) bad |= (f[i].y ^ tx) | ((f[i].x ^ tag.x) & mx);
        if (__all(bad == 0u)) break;
        __builtin_amdgcn_s_sleep(12);
      }
      __builtin_amdgcn_fence(__ATOMIC_ACQUIRE, "wavefront");
    };
    auto load_x = [&](v4u (&y)[8]) {
      STAMP(p, 14);
#pragma unroll
      for (int i = 0; i < 8; ++i)
        y[i] = __builtin_amdgcn_raw_buffer_load_b128(rs, WS_XT + (lane >> 1) * 8192 + (b & 7) * 1024 + (2 * (b >> 3) + (lane & 1)) * 16, i * 32 * 8192, SC1);
    };
    bool have_x = false;
    if (w < 6) tag = make_tag();
    else if (!isL) {
      if (w == 6) {
        if (!__all(((f[0].y ^ tag.x) | (f[1].y ^ tag.x) | (f[2].y ^ tag.x) | (f[3].y ^ tag.x)) == 0u)) poll4(tag.x, 0u);
#pragma unroll
        for (int i = 0; i < 4; ++i) *(LAS unsigned*)(sm + GR + i * 256 + lane * 4) = f[i].x;
        asm volatile("" ::: "memory");
        lds_set(sm, 8);
      } else if (FXP == 0) {
        unsigned bad = 0;
#pragma unroll
        for (int i = 0; i < 4; ++i) bad |= (f[i].y ^ tag.y) | (f[i].x ^ tag.x);
        have_x = __all(bad == 0u);
        __builtin_amdgcn_fence(__ATOMIC_ACQUIRE, "wavefront");
        if (have_x) load_x(xv);
      }
    }
    __builtin_amdgcn_sched_barrier(0);
    STAMP(p, 9);
    w2_load<W2_B, 28>(rw, woff, w, rl, wr);
    __builtin_amdgcn_sched_barrier(0);
    STAMP(p, 4);
    __builtin_amdgcn_sched_barrier(0);
    __builtin_amdgcn_s_setprio(EXP_PRIO);
    kexpand(sm, w - 4, lane, kix);
    __builtin_amdgcn_sched_barrier(0);
    STAMP(p, 15);
    auto stage_x = [&](const v4u (&y)[8]) {
#pragma unroll
      for (int i = 0; i < 8; ++i) *(LAS v4u*)(sm + CV_X + (lane & 1) * CV_XS + 512 + (i * 32 + (lane >> 1)) * 16) = y[i];
    };
    if (have_x) { stage_x(xv); asm volatile("; early x" ::: "memory"); }
    else if (w == 7) {
      v4u y[8];
      poll4(tag.y, ~0u);
      load_x(y);
      if (b == 0 && lane == 0) __builtin_amdgcn_raw_buffer_store_b64(tag, rs, WS_EPOCH, 0, SC1);
      stage_x(y);
      asm volatile("; late x" ::: "memory");
    }
    __builtin_amdgcn_s_setprio(0);
    asm volatile("s_waitcnt lgkmcnt(0)" ::: "memory");
    if (lane == 0) __hip_atomic_fetch_add((LAS unsigned*)(sm + SY) + 4, 1u, __ATOMIC_RELAXED, __HIP_MEMORY_SCOPE_WORKGROUP);
    __builtin_amdgcn_sched_barrier(0);
    w2_transpose(wr); w2_transpose(wr + 4); w2_transpose(wr + 8);
    __builtin_amdgcn_sched_barrier(0);
    STAMP(p, 29);
    if (!isL) lds_wait(sm, 8 + w);
    conv(p, ts, sm, rs, b, w - 4, lane, tag, wr);
    STAMP(p, 8);
    gemm2<false>(p, ts, sm, rs, rt2, ct2, rl, isL, w, lane, tag, tag, wr);
    finish();
  }
}

extern "C" void kernel_launch(void* const* d_in, const int* in_sizes, int n_in, void* d_out, int out_size, void* d_ws, size_t ws_size,
                              hipStream_t stream) {
  Params p;
  p.x = (const u16*)d_in[0]; p.lar = (const u16*)d_in[1]; p.aim = (const u16*)d_in[2]; p.cre = (const u16*)d_in[3];
  p.cim = (const u16*)d_in[4]; p.ldt = (const u16*)d_in[5]; p.dsk = (const u16*)d_in[6]; p.w1 = (const u16*)d_in[8];
  p.w2 = (const u16*)d_in[9]; p.out = (u16*)d_out; p.ws = (char*)d_ws;
  fused_kernel<<<256, 512, 0, stream>>>(p);
}
```

```cpp
#include <hip/hip_runtime.h>
#include <hip/hip_bf16.h>
#include <stdint.h>

typedef unsigned short u16;
typedef unsigned long long u64;
typedef unsigned v4u __attribute__((ext_vector_type(4)));
typedef unsigned v2u __attribute__((ext_vector_type(2)));
typedef float f32x2 __attribute__((ext_vector_type(2)));
typedef float f32x4 __attribute__((ext_vector_type(4)));
typedef float f32x16 __attribute__((ext_vector_type(16)));
typedef short s16x4 __attribute__((ext_vector_type(4)));
typedef __bf16 bf16x2 __attribute__((ext_vector_type(2)));
typedef __bf16 bf16x8 __attribute__((ext_vector_type(8)));
typedef _Float16 f16x2 __attribute__((ext_vector_type(2)));
typedef _Float16 f16x8 __attribute__((ext_vector_type(8)));
typedef __amdgpu_buffer_rsrc_t rsrc_t;
#define GAS __attribute__((address_space(1)))
#define LAS __attribute__((address_space(3)))
#define DEV __device__ __forceinline__

constexpr int L = 2048, D = 512, NM = 128, F = 1792;
constexpr int WS_EPOCH = 0;
constexpr int FSTR = 64;
constexpr int WS_FH = 4096;
constexpr int WS_FX = WS_FH + 256 * FSTR;
constexpr int WS_FS = WS_FX + 256 * FSTR;
constexpr int WS_GS = WS_FS + 256 * FSTR;
constexpr int WS_GC = WS_GS + L * 8;
constexpr int WS_XT = 1 << 17;
constexpr int WS_ST = WS_XT + (2 << 20);
constexpr int WS_HF = WS_ST + (2 << 20);
constexpr int WS_BYTES = WS_HF + L * F * 2;
constexpr int SC1 = 16;
constexpr int LOADER_DELAY = 5;
constexpr int FXS = 16;
constexpr int W2_A = 4;
constexpr int W2_B = 14;
constexpr int EXP_PRIO = 2;
constexpr int MAIN_PRE_T = 4;
constexpr int CODE_BYTES = 35 * 1024;
constexpr unsigned SPIN_CAP = 1u << 16;
constexpr int G1_SLOT = 32768, G1_R = 4;
constexpr int KG_ROW = 272;
constexpr int KG_WAVE = 53 * KG_ROW;
constexpr int KG_TAB = 3 * G1_SLOT;
constexpr int KG_K = KG_TAB + 4 * KG_WAVE;
constexpr int KG_KCH = 676 * 4;
constexpr int SY = KG_K + 2 * KG_KCH;
constexpr int GR = SY + 128;
constexpr int LDS_USED = GR + 1024;
constexpr int CV_KS = 4288;
constexpr int CV_K = 0;
constexpr int CV_X = 8 * CV_KS;
constexpr int CV_XS = (256 + 2048 + 16) * 2;
constexpr int CV_E = CV_X + 2 * CV_XS;
constexpr int G2_S = 4 * G1_SLOT;
constexpr int LDS_BYTES = 160 * 1024;
static_assert(WS_GC + F * 8 <= WS_XT && G2_S + 64 * 144 + 8192 <= KG_K && LDS_USED <= LDS_BYTES && CV_E % 16 == 0 && CV_E + 16384 <= 2 * G1_SLOT, "maps");

DEV float bf2f(unsigned v) { return __uint_as_float(v << 16); }
DEV float bflo(unsigned v) { return __uint_as_float(v << 16); }
DEV float bfhi(unsigned v) { return __uint_as_float(v & 0xffff0000u); }
DEV unsigned pk_bf16(float a, float b) { f32x2 f = {a, b}; return __builtin_bit_cast(unsigned, __builtin_convertvector(f, bf16x2)); }
DEV unsigned pk_f16(float a, float b) { f32x2 f = {a, b}; return __builtin_bit_cast(unsigned, __builtin_convertvector(f, f16x2)); }
DEV float rbf(float a) { return bflo(pk_bf16(a, a) & 0xffffu); }
DEV float dot2bf(unsigned a, unsigned b, float c) {
  return __builtin_amdgcn_fdot2_f32_bf16(__builtin_bit_cast(bf16x2, a), __builtin_bit_cast(bf16x2, b), c, false);
}
DEV void lds_barrier() { asm volatile("s_waitcnt lgkmcnt(0)" ::: "memory"); __builtin_amdgcn_s_barrier(); asm volatile("" ::: "memory"); }
DEV void drain() { asm volatile("s_waitcnt vmcnt(0)" ::: "memory"); }
template <int CTRL> DEV float dpp_add(float v) {
  return v + __builtin_bit_cast(float, __builtin_amdgcn_update_dpp(0, __builtin_bit_cast(int, v), CTRL, 0xf, 0xf, true));
}
DEV float row16_sum(float v) { v = dpp_add<0xb1>(v); v = dpp_add<0x4e>(v); v = dpp_add<0x124>(v); return dpp_add<0x128>(v); }
DEV float swap32_add(float a, float b) {
  auto r = __builtin_amdgcn_permlane32_swap(__float_as_uint(a), __float_as_uint(b), false, false);
  return __uint_as_float(r[0]) + __uint_as_float(r[1]);
}
DEV float swap16_add(float a, float b) {
  auto r = __builtin_amdgcn_permlane16_swap(__float_as_uint(a), __float_as_uint(b), false, false);
  return __uint_as_float(r[0]) + __uint_as_float(r[1]);
}
DEV float rdlane(float v, int l) { return __uint_as_float(__builtin_amdgcn_readlane(__float_as_uint(v), l)); }
template <int I> struct IC { static constexpr int v = I; };
template <int B, int E, class Fn> DEV void static_for(Fn&& f) { if constexpr (B < E) { f(IC<B>{}); static_for<B + 1, E>(f); } }

struct Params {
  const u16 *x, *lar, *aim, *cre, *cim, *ldt, *dsk, *w1, *w2;
  u16* out;
  char* ws;
};

DEV void wait_flags(rsrc_t rs, int off, int n, v2u tag, int lane) {
#pragma unroll 1
  for (unsigned spins = 0; spins < SPIN_CAP; ++spins) {
    asm volatile("" ::: "memory");
    const v2u v = __builtin_amdgcn_raw_buffer_load_b64(rs, off + min(lane, n - 1) * FSTR, 0, SC1);
    if (__all(v.x == tag.x && v.y == tag.y)) break;
    __builtin_amdgcn_s_sleep(4);
  }
  __builtin_amdgcn_fence(__ATOMIC_ACQUIRE, "wavefront");
}
DEV void publish(rsrc_t rs, LAS char* sm, int word, unsigned nwaves, int off, v2u tag, int lane) {
  drain();
  if (lane == 0) {
    const unsigned old = __hip_atomic_fetch_add((LAS unsigned*)(sm + SY) + word, 1u, __ATOMIC_RELAXED, __HIP_MEMORY_SCOPE_WORKGROUP);
    if (old == nwaves - 1) __builtin_amdgcn_raw_buffer_store_b64(tag, rs, off, 0, SC1);
  }
}
DEV void wave_sync(LAS char* sm, int word, unsigned n) {
  asm volatile("s_waitcnt lgkmcnt(0)" ::: "memory");
  volatile LAS unsigned* c = (volatile LAS unsigned*)(sm + SY) + word;
  if ((threadIdx.x & 63) == 0) __hip_atomic_fetch_add((LAS unsigned*)c, 1u, __ATOMIC_RELAXED, __HIP_MEMORY_SCOPE_WORKGROUP);
#pragma unroll 1
  for (unsigned spins = 0; spins < 16 * SPIN_CAP && __builtin_amdgcn_readfirstlane(*c) != n; ++spins) {}
  asm volatile("" ::: "memory");
}

DEV void lds_set(LAS char* sm, int word) { *((volatile LAS unsigned*)(sm + SY) + word) = 1u; }
DEV void lds_wait(LAS char* sm, int word, unsigned want = 1u) {
  volatile LAS unsigned* c = (volatile LAS unsigned*)(sm + SY) + word;
#pragma unroll 1
  for (unsigned spins = 0; spins < 16 * SPIN_CAP && __builtin_amdgcn_readfirstlane(*c) != want; ++spins) __builtin_amdgcn_s_sleep(1);
  __builtin_amdgcn_fence(__ATOMIC_ACQUIRE, "wavefront");
  asm volatile("" ::: "memory");
}
DEV void bar() { __builtin_amdgcn_sched_barrier(0); asm volatile("" ::: "memory"); __builtin_amdgcn_s_barrier(); asm volatile("" ::: "memory"); __builtin_amdgcn_sched_barrier(0); }

DEV void ln_wave(const Params& p, rsrc_t rs, int t8, int lane, v2u tag, const v4u (&xr)[8], v4u (&o)[8]) {
  float s[8], q[8], t[4], rz[2], mz[2];
#pragma unroll
  for (int r = 0; r < 8; ++r) {
    s[r] = 0.f; q[r] = 0.f;
#pragma unroll
    for (int j = 0; j < 4; ++j) { s[r] = dot2bf(xr[r][j], 0x3f803f80u, s[r]); q[r] = dot2bf(xr[r][j], xr[r][j], q[r]); }
  }
#pragma unroll
  for (int j = 0; j < 4; ++j) { t[j] = swap32_add(s[j], s[j + 4]); q[j] = swap32_add(q[j], q[j + 4]); }
#pragma unroll
  for (int u = 0; u < 2; ++u) {
    const float mean = row16_sum(swap16_add(t[u], t[u + 2])) * (1.f / D);
    const float var = rbf(fmaxf(row16_sum(swap16_add(q[u], q[u + 2])) * (1.f / D) - mean * mean, 0.f));
    const unsigned ms = pk_bf16(mean, __builtin_amdgcn_sqrtf(rbf(var + 1e-5f)));
    if ((lane & 15) == 0) __builtin_amdgcn_raw_buffer_store_b64((v2u){ms, tag.x}, rs, WS_GS + (t8 * 8 + u + 2 * (lane >> 4)) * 8, 0, SC1);
    rz[u] = __builtin_amdgcn_rcpf(bfhi(ms));
    mz[u] = -bflo(ms) * rz[u];
  }
  float rr[8], mm[8];
#pragma unroll
  for (int r = 0; r < 8; ++r) { rr[r] = rdlane(rz[r & 1], 16 * (r >> 1)); mm[r] = rdlane(mz[r & 1], 16 * (r >> 1)); }
#pragma unroll
  for (int j = 0; j < 4; ++j) {
    f32x2 v[8];
#pragma unroll
    for (int r = 0; r < 8; ++r) v[r] = __builtin_elementwise_fma((f32x2){bflo(xr[r][j]), bfhi(xr[r][j])}, (f32x2){rr[r], rr[r]}, (f32x2){mm[r], mm[r]});
#pragma unroll
    for (int e = 0; e < 2; ++e) {
#pragma unroll
      for (int i = 0; i < 4; ++i) o[2 * j + e][i] = pk_f16(v[2 * i][e], v[2 * i + 1][e]);
      asm volatile("" : "+v"(o[2 * j + e]));
    }
  }
}

DEV void cs_part(const Params& p, LAS char* sm, int cb, int hw, int lane) {
  v4u wr[32];
#pragma unroll
  for (int i = 0; i < 32; ++i) wr[i] = *(const GAS v4u*)((const GAS char*)p.w1 + (size_t)(hw * 256 + i * 8 + (lane >> 3)) * (F * 2) + cb * 128 + (lane & 7) * 16);
  __builtin_amdgcn_sched_barrier(0);
  unsigned sel_lo = 0x00003f80u, sel_hi = 0x3f800000u;
  asm volatile("" : "+s"(sel_lo), "+s"(sel_hi));
  float c[8] = {0.f, 0.f, 0.f, 0.f, 0.f, 0.f, 0.f, 0.f}, t[4], y[2];
#pragma unroll
  for (int i = 0; i < 32; ++i)
#pragma unroll
    for (int j = 0; j < 4; ++j) { c[2 * j] = dot2bf(sel_lo, wr[i][j], c[2 * j]); c[2 * j + 1] = dot2bf(sel_hi, wr[i][j], c[2 * j + 1]); }
#pragma unroll
  for (int j = 0; j < 4; ++j) t[j] = swap32_add(c[j], c[j + 4]);
#pragma unroll
  for (int u = 0; u < 2; ++u) y[u] = dpp_add<0x128>(swap16_add(t[u], t[u + 2]));
  if ((lane & 8) == 0) *(LAS f32x2*)(sm + 65536 + (hw * 64 + (lane & 7) * 8 + 2 * (lane >> 4)) * 4) = (f32x2){y[0], y[1]};
}

struct KPar { unsigned ldt, lar, aim, c0, c1, c2, c3, dsk; };
DEV int chan_of(int b, int ch) { return 16 * (b >> 3) + 8 * ch + (b & 7); }
DEV f32x2 crot(f32x2 w) { return (f32x2){-w.y, w.x}; }
DEV f32x2 cmul(f32x2 a, f32x2 w, f32x2 wr) { return __builtin_elementwise_fma(a.yy, wr, a.xx * w); }
DEV f32x2 cexp_at(float a2, float rv, float pos) {
  const float e = __builtin_amdgcn_exp2f(a2 * pos);
  const float ph = __builtin_amdgcn_fractf(rv * pos);
  return (f32x2){__builtin_amdgcn_cosf(ph), __builtin_amdgcn_sinf(ph)} * e;
}
DEV float exp_acc(float x) {
  const float t = x * 1.44269504088896f;
  const float r = fmaf(x, 1.92596299112661e-8f, fmaf(x, 1.44269504088896f, -t));
  const float e = __builtin_amdgcn_exp2f(t);
  return fmaf(e, r * 0.693147180559945f, e);
}
DEV void kgen(const Params& p, LAS char* sm, const KPar& k, int w, int lane) {
  const int ch = w >> 1;
  const float dt = rbf(exp_acc(bf2f(k.ldt)));
  const float a2 = -rbf(exp_acc(bf2f(k.lar))) * dt * 1.44269504088896f;
  const float rv = bf2f(k.aim) * dt * 0.159154943091895f;
  LAS char* t = sm + KG_TAB + w * KG_WAVE + lane * 4;
  auto put = [&](int row, f32x2 u) { *(LAS unsigned*)(t + row * KG_ROW) = pk_f16(u.x, u.y); };
  {
    const f32x2 w8 = cexp_at(a2, -rv, 8.f), w8r = crot(w8);
    const f32x2 w64 = cexp_at(a2, -rv, 64.f), w64r = crot(w64), w128 = cmul(w64, w64, w64r);
    f32x2 v[4] = {{1.f, 0.f}, w64, w128, cmul(w128, w64, w64r)};
#pragma unroll
    for (int i = 0; i < 8; ++i)
#pragma unroll
      for (int c = 0; c < 4; ++c) { put(21 + 8 * c + i, v[c]); if (i < 7) v[c] = cmul(v[c], w8, w8r); }
  }
  {
    unsigned mx = max(max(k.c0 & 0x7fffu, k.c1 & 0x7fffu), max(k.c2 & 0x7fffu, k.c3 & 0x7fffu));
    mx = max(mx, (unsigned)__builtin_amdgcn_update_dpp(0, (int)mx, 0xb1, 0xf, 0xf, true));
    mx = max(mx, (unsigned)__builtin_amdgcn_update_dpp(0, (int)mx, 0x4e, 0xf, 0xf, true));
    mx = max(mx, (unsigned)__builtin_amdgcn_update_dpp(0, (int)mx, 0x124, 0xf, 0xf, true));
    mx = max(mx, (unsigned)__builtin_amdgcn_update_dpp(0, (int)mx, 0x128, 0xf, 0xf, true));
    unsigned ex = max(max(__builtin_amdgcn_readlane(mx, 0), __builtin_amdgcn_readlane(mx, 16)), max(__builtin_amdgcn_readlane(mx, 32), __builtin_amdgcn_readlane(mx, 48))) >> 7;
    ex = min(max(ex, 1u), 253u);
    const float sc = __uint_as_float((254u - ex) << 23), isc = __uint_as_float(ex << 23);
    if (lane == 0) *(LAS f32x2*)(sm + SY + 64 + ch * 8) = (f32x2){2.f * dt * isc, bf2f(k.dsk)};
    const f32x2 c = (f32x2){bf2f(k.c0), bf2f(k.c1)} * sc;
    const f32x2 w1 = cexp_at(a2, rv, 1.f), e256 = cexp_at(a2, rv, 256.f), e256r = crot(e256), e512 = cmul(e256, e256, e256r), e1024 = cmul(e512, e512, crot(e512));
    const f32x2 w1r = crot(w1), w2 = cmul(w1, w1, w1r), w2r = crot(w2), w4 = cmul(w2, w2, w2r), w4r = crot(w4), cr = crot(c);
    f32x2 u = c, u4 = cmul(c, w4, w4r), u2 = cmul(e256, c, cr);
#pragma unroll
    for (int i = 0; i < 4; ++i) {
      put(i, u); put(4 + i, u4); put(8 + i, u2);
      if (i < 3) { u = cmul(u, w1, w1r); u4 = cmul(u4, w1, w1r); u2 = cmul(u2, w2, w2r); }
    }
    const f32x2 u512 = cmul(e512, c, cr), u768 = cmul(u512, e256, e256r), u1024 = cmul(e1024, c, cr), u1024r = crot(u1024);
    const f32x2 u1536 = cmul(e512, u1024, u1024r);
    put(12, u512); put(13, cmul(u512, w4, w4r)); put(14, u768); put(15, cmul(u768, w4, w4r));
    put(16, u1024); put(17, cmul(u1024, e256, e256r)); put(18, u1536); put(19, cmul(u1536, e256, e256r)); put(20, cmul(e1024, u1024, u1024r));
  }
  asm volatile("" ::: "memory");
  {
    const int h = lane >> 5, b = lane & 31;
    const LAS char* ua = sm + KG_TAB + w * KG_WAVE + b * KG_ROW + h * 16;
    f32x16 acc = {};
    f16x8 fa[8], fb[8];
#pragma unroll
    for (int s = 0; s < 8; ++s) { fa[s] = *(const LAS f16x8*)(ua + s * 32); fb[s] = *(const LAS f16x8*)(ua + 21 * KG_ROW + s * 32); }
    __builtin_amdgcn_sched_barrier(0);
#pragma unroll
    for (int s = 0; s < 8; ++s) acc = __builtin_amdgcn_mfma_f32_32x32x16_f16(fa[s], fb[s], acc, 0, 0, 0);
    LAS _Float16* kt = (LAS _Float16*)(sm + KG_K + ch * KG_KCH) + 8 + (w & 1);
    auto add = [&](int i, float v) { kt[2 * i] = (_Float16)v; };
    const int i0 = 4 * h + 8 * b, i1 = h ? 384 + 2 * b : 256 + 4 * b, i1s = h ? 64 : 2;
#pragma unroll
    for (int r = 0; r < 4; ++r) add(i0 + r, acc[r]);
    add(i1, acc[4]); add(i1 + 1, acc[5]); add(i1 + i1s, acc[6]); add(i1 + i1s + 1, acc[7]);
    add(512 + 128 * h + b, acc[8]);
    if (h == 0) { add(544 + b, acc[9]); add(576 + b, acc[10]); add(608 + b, acc[11]); }
  }
}
struct KIdx { unsigned a[11]; };
DEV KIdx kexpand_idx(int hw, int lane) {
  KIdx ix;
  const int pp = (hw & 1) * 64 + lane;
#pragma unroll
  for (int i = 0; i < 11; ++i) {
    const int lag = 8 * pp - 3 + i;
    const int code = (int)(pk_bf16((float)lag, 0.f) & 0xffffu) - 0x4280;
    ix.a[i] = KG_K + (hw >> 1) * KG_KCH + 16 + 4 * (lag < 128 ? lag : code);
    asm volatile("" : "+v"(ix.a[i]));
  }
  return ix;
}
DEV void kexpand(LAS char* sm, int hw, int lane, const KIdx& ix) {
  const int ch = hw >> 1;
  const LAS unsigned* kt = (const LAS unsigned*)(sm + KG_K + ch * KG_KCH) + 4;
  LAS char* kc = sm + CV_K + ch * 4 * CV_KS;
  auto sum2 = [&](unsigned hi, unsigned lo) {
    const unsigned a = __builtin_amdgcn_perm(hi, lo, 0x05040100u), b = __builtin_amdgcn_perm(hi, lo, 0x07060302u);
    return __builtin_bit_cast(unsigned, __builtin_bit_cast(f16x2, a) + __builtin_bit_cast(f16x2, b));
  };
  auto emit = [&](int pp, const unsigned (&pr)[10]) {
#pragma unroll
    for (int sg = 0; sg < 4; ++sg) *(LAS v4u*)(kc + sg * CV_KS + (516 - 2 * pp) * 8) = (v4u){pr[6 + sg], pr[4 + sg], pr[2 + sg], pr[sg]};
  };
  const int p3 = 128 + (hw & 1) * 64 + lane;
  const unsigned ka = kt[384 + p3], kb = kt[385 + p3], k0 = kt[p3 == 128 ? 511 : 384 + p3];
  {
    const int pp = (hw & 1) * 64 + lane;
    unsigned kv[12], pr[10], ev[6];
#pragma unroll
    for (int i = 0; i < 11; ++i) kv[i] = *(const LAS unsigned*)(sm + ix.a[i]);
    kv[11] = kv[10];
#pragma unroll
    for (int i = 0; i < 6; ++i) ev[i] = sum2(kv[2 * i], kv[2 * i + 1]);
#pragma unroll
    for (int i = 0; i < 5; ++i) { pr[2 * i] = ev[i]; pr[2 * i + 1] = __builtin_amdgcn_alignbit(ev[i], ev[i + 1], 16); }
    emit(pp, pr);
  }
  {
    const int pp = p3;
    const unsigned ab = sum2(ka, kb);
    const unsigned a0 = sum2(k0, ka);
    const unsigned aa = __builtin_amdgcn_perm(ab, ab, 0x03020302u), bb = __builtin_amdgcn_perm(ab, ab, 0x01000100u);
    const unsigned pr[10] = {a0, aa, aa, aa, aa, aa, (pp & 1) ? ab : aa, (pp & 1) ? bb : ab, bb, bb};
    emit(pp, pr);
  }
  if (lane < 20) *(LAS v4u*)(lane < 4 ? kc + ((hw & 1) * 2 + (lane >> 1)) * CV_KS + (518 + 2 * (lane & 1)) * 8 : sm + CV_X + ch * CV_XS + ((hw & 1) * 16 + lane - 4) * 16) = (v4u){0u, 0u, 0u, 0u};
}

DEV int ks_of(int w, int rl, int i) { const int k = i + (rl >> 3); return 7 * ((w + rl) & 7) + (k >= 7 ? k - 7 : k); }
template <int LO, int HI> DEV void w2_load(rsrc_t rw, int woff, int w, int rl, v4u (&wr)[28]) {
#pragma unroll
  for (int i = LO; i < HI; ++i) wr[i] = __builtin_amdgcn_raw_buffer_load_b128(rw, woff + (i & 3) * 1024, ks_of(w, rl, i >> 2) * 32768, 0);
}
DEV void w2_transpose(v4u* r) {
  unsigned P[8], Q[8];
  asm("s_mov_b64 vcc, %32\n\ts_nop 0\n\t"
#define CN(d, a, b) "v_cndmask_b32_dpp %" #d ", %" #a ", %" #b ", vcc row_ror:8 row_mask:0xf bank_mask:0xf\n\t"
      CN(0, 18, 16) CN(1, 19, 17) CN(2, 22, 20) CN(3, 23, 21) CN(4, 26, 24) CN(5, 27, 25) CN(6, 30, 28) CN(7, 31, 29)
      "s_not_b64 vcc, vcc\n\t"
      CN(8, 16, 18) CN(9, 17, 19) CN(10, 20, 22) CN(11, 21, 23) CN(12, 24, 26) CN(13, 25, 27) CN(14, 28, 30) CN(15, 29, 31)
#undef CN
      : "=&v"(P[0]), "=&v"(P[1]), "=&v"(P[2]), "=&v"(P[3]), "=&v"(P[4]), "=&v"(P[5]), "=&v"(P[6]), "=&v"(P[7]),
        "=&v"(Q[0]), "=&v"(Q[1]), "=&v"(Q[2]), "=&v"(Q[3]), "=&v"(Q[4]), "=&v"(Q[5]), "=&v"(Q[6]), "=&v"(Q[7])
      : "v"(r[0][0]), "v"(r[0][1]), "v"(r[0][2]), "v"(r[0][3]), "v"(r[1][0]), "v"(r[1][1]), "v"(r[1][2]), "v"(r[1][3]),
        "v"(r[2][0]), "v"(r[2][1]), "v"(r[2][2]), "v"(r[2][3]), "v"(r[3][0]), "v"(r[3][1]), "v"(r[3][2]), "v"(r[3][3]), "s"(0x00ff00ff00ff00ffull)
      : "vcc", "scc");
#pragma unroll
  for (int t = 0; t < 4; ++t) {
    const unsigned sel = (t & 1) ? 0x07060302u : 0x05040100u;
    const int u = t >> 1;
    r[t] = (v4u){__builtin_amdgcn_perm(P[2 + u], P[u], sel), __builtin_amdgcn_perm(P[6 + u], P[4 + u], sel),
                 __builtin_amdgcn_perm(Q[2 + u], Q[u], sel), __builtin_amdgcn_perm(Q[6 + u], Q[4 + u], sel)};
  }
  asm volatile("" : "+v"(r[0]), "+v"(r[1]), "+v"(r[2]), "+v"(r[3]));
}

struct Loader { const char* gb; int cstep, gstep, rot; unsigned slot, v[4]; };
DEV Loader loader_init(const Params& p, LAS char* sm, int rt, int ct, int rot, int hw, int lane) {
  Loader ld;
  const u64 m = -(u64)hw;
  ld.gb = (const char*)((((u64)p.w1 + ct * 256) & m) | (((u64)p.x + rt * 128 * 1024) & ~m));
  ld.cstep = 128 + hw * (64 * F * 2 - 128);
  ld.gstep = 32768 + hw * (16 * F * 2 - 32768);
  ld.rot = rot;
  ld.slot = (unsigned)(size_t)sm + hw * 16384;
  const unsigned base = (lane >> (3 + hw)) * (1024 + hw * (F * 2 - 1024)) + (((lane & (7 + 8 * hw)) ^ ((lane >> 4) << (2 * hw))) << 4);
#pragma unroll
  for (int i = 0; i < 4; ++i) ld.v[i] = (base ^ (((i & 1) << 6) + hw * (((i & 2) << 4) - ((i & 1) << 6)))) + i * (ld.gstep >> 2);
  return ld;
}
DEV void loader_issue(const Loader& ld, int c) {
  const char* g0 = ld.gb + (size_t)(((c + ld.rot) & 7) * ld.cstep);
  const char *g1 = g0 + ld.gstep, *g2 = g1 + ld.gstep, *g3 = g2 + ld.gstep;
#define GL4(S) "global_load_lds_dwordx4 %0, " S "\n\ts_add_u32 m0, m0, 0x400\n\ts_nop 0\n\tglobal_load_lds_dwordx4 %1, " S "\n\ts_add_u32 m0, m0, 0x400\n\ts_nop 0\n\t" \
               "global_load_lds_dwordx4 %2, " S "\n\ts_add_u32 m0, m0, 0x400\n\ts_nop 0\n\tglobal_load_lds_dwordx4 %3, " S "\n\ts_add_u32 m0, m0, 0x400\n\ts_nop 0\n\t"
  asm volatile("s_mov_b32 m0, %8\n\ts_nop 0\n\t" GL4("%4") GL4("%5") GL4("%6") GL4("%7")
               :: "v"(ld.v[0]), "v"(ld.v[1]), "v"(ld.v[2]), "v"(ld.v[3]), "s"(g0), "s"(g1), "s"(g2), "s"(g3), "s"(__builtin_amdgcn_readfirstlane(ld.slot + (c & 3) * G1_SLOT)) : "memory", "scc");
#undef GL4
}
struct G1Frag { v4u a[4], b[4]; };
struct G1Base { LAS char *a, *b; };
DEV G1Base g1_base(LAS char* sm, int w, int lane) {
  const int wf = w & 1, wt = w >> 1, g = lane >> 4, r16 = lane & 15, q4 = r16 >> 2, p4 = lane & 3, tok0 = wt * 64 + r16;
  G1Base r;
  r.a = sm + 16384 + (8 * g + q4) * 256 + (((8 * wf + (p4 >> 1)) ^ (q4 << 2) ^ ((g & 1) << 1)) << 4) + 8 * (p4 & 1);
  r.b = sm + tok0 * 128 + ((g ^ ((tok0 >> 1) & 7)) << 4);
  asm volatile("" : "+v"(r.a), "+v"(r.b));
  return r;
}
DEV void g1_main(const Params& p, LAS char* sm, rsrc_t rs, int rt, int ct, int w, int lane, v2u tag, G1Base gb) {
  const int wf = w & 1, wt = w >> 1, g = lane >> 4, r16 = lane & 15, tok0 = wt * 64 + r16;
  f32x4 acc[4][4] = {};
  LAS char *pa[2][4], *pb[2][2];
#pragma unroll
  for (int i = 0; i < 4; ++i) { pa[0][i] = (LAS char*)((size_t)gb.a ^ (i << 5)); asm volatile("" : "+v"(pa[0][i])); }
#pragma unroll
  for (int s = 0; s < 2; ++s) { pb[0][s] = (LAS char*)((size_t)gb.b ^ (s << 6)); asm volatile("" : "+v"(pb[0][s])); }
  G1Frag fr[16];
  auto rd = [&](int n) {
#pragma unroll
    for (int i = 0; i < 4; ++i) {
      LAS char* o = pa[(n >> 2) & 1][i] + ((n >> 1) & 1) * G1_SLOT + (n & 1) * 8192;
      const v2u a0 = __builtin_bit_cast(v2u, __builtin_amdgcn_ds_read_tr16_b64_v4i16((LAS s16x4*)o));
      const v2u a1 = __builtin_bit_cast(v2u, __builtin_amdgcn_ds_read_tr16_b64_v4i16((LAS s16x4*)(o + 1024)));
      fr[n].a[i] = (v4u){a0.x, a0.y, a1.x, a1.y};
      fr[n].b[i] = *(const LAS v4u*)(pb[(n >> 2) & 1][n & 1] + ((n >> 1) & 1) * G1_SLOT + 2048 * i);
    }
  };
  float nm[4], sd[4], c1[4];
  f32x4 cs[4];
  unsigned gs[4];
  static_for<-1, 16>([&](auto sn) {
    constexpr int st = decltype(sn)::v;
    if constexpr (st < 12 && ((st + 1) & 1) == 0) lds_barrier();
    if constexpr (st < 15) rd(st + 1);
    if constexpr (st == -1) {
#pragma unroll
      for (int i = 0; i < 4; ++i) { pa[1][i] = pa[0][i] + 65536; asm volatile("" : "+v"(pa[1][i])); }
#pragma unroll
      for (int s = 0; s < 2; ++s) { pb[1][s] = pb[0][s] + 65536; asm volatile("" : "+v"(pb[1][s])); }
    }
    if constexpr (st == 15) {
      lds_wait(sm, 8);
#pragma unroll
      for (int i = 0; i < 4; ++i) {
        gs[i] = *(const LAS unsigned*)(sm + GR + (tok0 + 16 * i) * 4);
        cs[i] = *(const LAS f32x4*)(sm + GR + 512 + (wf * 64 + 16 * i + 4 * g) * 4);
      }
#pragma unroll
      for (int j = 0; j < 4; ++j) { nm[j] = -bflo(gs[j]); sd[j] = bfhi(gs[j]); c1[j] = __builtin_amdgcn_rcpf(sd[j]) * -1.44269504088896f; }
    }
    if constexpr (st >= 0) {
#pragma unroll
      for (int i2 = 0; i2 < 2; ++i2)
#pragma unroll
        for (int j = 0; j < 4; ++j)
#pragma unroll
          for (int i = 2 * i2; i < 2 * i2 + 2; ++i)
            acc[i][j] = __builtin_amdgcn_mfma_f32_16x16x32_bf16(__builtin_bit_cast(bf16x8, fr[st].a[i]), __builtin_bit_cast(bf16x8, fr[st].b[j]), acc[i][j], 0, 0, 0);
#pragma unroll
      for (int i = 0; i < 16; ++i) {
        __builtin_amdgcn_sched_group_barrier(0x8, 1, 0);
        if (i < 12) __builtin_amdgcn_sched_group_barrier(0x100, 1, 0); else __builtin_amdgcn_sched_group_barrier(0x402, 4, 0);
      }
    }
    __builtin_amdgcn_sched_barrier(0);
  });
  auto act = [&](int i, int j) {
    const f32x4 t = __builtin_elementwise_fma(cs[i], (f32x4){nm[j], nm[j], nm[j], nm[j]}, acc[i][j]);
    const f32x4 u = t * c1[j];
    const f32x4 ex = {__builtin_amdgcn_exp2f(u[0]), __builtin_amdgcn_exp2f(u[1]), __builtin_amdgcn_exp2f(u[2]), __builtin_amdgcn_exp2f(u[3])};
    const f32x4 dn = __builtin_elementwise_fma(ex, (f32x4){sd[j], sd[j], sd[j], sd[j]}, (f32x4){sd[j], sd[j], sd[j], sd[j]});
    const f32x4 r = t * (f32x4){__builtin_amdgcn_rcpf(dn[0]), __builtin_amdgcn_rcpf(dn[1]), __builtin_amdgcn_rcpf(dn[2]), __builtin_amdgcn_rcpf(dn[3])};
    return (v2u){pk_bf16(r[0], r[1]), pk_bf16(r[2], r[3])};
  };
  const int hbase = WS_HF + ((2 * rt + wt) * 56 + 4 * ct + 2 * wf) * 4096 + lane * 16;
#pragma unroll
  for (int u = 0; u < 8; ++u) {
    const v2u l2 = act(2 * (u >> 2), u & 3), h2 = act(2 * (u >> 2) + 1, u & 3);
    __builtin_amdgcn_raw_buffer_store_b128((v4u){l2.x, l2.y, h2.x, h2.y}, rs, hbase + u * 1024, 0, SC1);
    __builtin_amdgcn_sched_barrier(0);
  }
  lds_set(sm, 12 + w);
  drain();
  if (lane == 0) {
    const unsigned old = __hip_atomic_fetch_add((LAS unsigned*)(sm + SY) + 1, 1u, __ATOMIC_RELAXED, __HIP_MEMORY_SCOPE_WORKGROUP);
    if (old == 3) __builtin_amdgcn_raw_buffer_store_b64(tag, rs, WS_FH + (rt * 16 + ct) * FSTR, 0, SC1);
  }
  __builtin_amdgcn_s_setprio(0);
}

DEV void conv(const Params& p, LAS char* sm, rsrc_t rs, int b, int hw, int lane, v2u tag, v4u (&wr)[28]) {
  const int ch = hw >> 1, eh = hw & 1, g = lane >> 4, r16 = lane & 15, d = chan_of(b, ch);
  LAS char* xs = sm + CV_X + ch * CV_XS;
  lds_wait(sm, 4, 4u);
  const LAS char* ka = sm + CV_K + (ch * 4 + (r16 & 3)) * CV_KS + (517 - (r16 >> 2) + 2 * g) * 8;
  const LAS char* xa = xs + 512 + r16 * 32 + g * 16;
  auto lda = [&](const LAS char* ptr) {
    const v2u lo = *(const volatile LAS v2u*)ptr, hi = *(const volatile LAS v2u*)(ptr + 8);
    return __builtin_bit_cast(f16x8, (v4u){lo.x, lo.y, hi.x, hi.y});
  };
  f32x4 acc[8];
#pragma unroll
  for (int a = 0; a < 8; ++a) acc[a] = (f32x4){0.f, 0.f, 0.f, 0.f};
  const LAS char* ke = ka - 256 * eh - 3584 - 256;
  const LAS char* xe = xa - 256 * eh - 256;
  f16x8 A[4][8], B[4][8];
  auto ld = [&](int e, int i) { if (e < 4) { A[e][i] = lda(ke + 3584 - 512 * i + 224 - 64 * e); B[e][i] = *(const LAS f16x8*)(xe + 224 - 64 * e + 512 * i); } };
  ld(0, 0); ld(0, 7); ld(0, 1); ld(0, 6); ld(0, 2); ld(0, 5); ld(0, 3); ld(0, 4);
  __builtin_amdgcn_sched_barrier(0);
  static_for<0, 4>([&](auto ee) {
    constexpr int e = decltype(ee)::v;
    static_for<0, 8>([&](auto ll) {
      constexpr int lv = 7 - decltype(ll)::v;
      constexpr int n = (7 - lv < lv ? 7 - lv : lv - 1) + 1;
      if constexpr (lv == 4) ld(e + 1, 0);
      if constexpr (lv == 3) ld(e + 1, 1);
      if constexpr (lv < 7 && lv >= 1) ld(e + 1, lv + 1);
#pragma unroll
      for (int i = 0; i < n; ++i) {
        acc[lv + i] = __builtin_amdgcn_mfma_f32_16x16x32_f16(A[e][i], B[e][lv], acc[lv + i], 0, 0, 0);
        acc[lv + i] = __builtin_amdgcn_mfma_f32_16x16x32_f16(A[e][lv], B[e][i], acc[lv + i], 0, 0, 0);
      }
      if constexpr (2 * lv <= 7) acc[2 * lv] = __builtin_amdgcn_mfma_f32_16x16x32_f16(A[e][lv], B[e][lv], acc[2 * lv], 0, 0, 0);
#pragma unroll
      for (int i = 0; i < 6; ++i) { __builtin_amdgcn_sched_group_barrier(0x8, 1, 0); __builtin_amdgcn_sched_group_barrier(0x100, 1, 0); }
      __builtin_amdgcn_sched_barrier(0);
    });
  });
  __builtin_amdgcn_s_setprio(2);
  LAS f32x4* ex = (LAS f32x4*)(sm + CV_E);
  const f32x2 md = *(const LAS f32x2*)(sm + SY + 64 + ch * 8);
  const int t0 = 1024 * eh + 16 * r16 + 4 * g;
  v2u xr[4];
#pragma unroll
  for (int i = 0; i < 4; ++i) xr[i] = *(const LAS v2u*)(xs + 512 + (t0 + 256 * i) * 2);
  f32x4 own[4];
  if (eh) {
#pragma unroll
    for (int i = 0; i < 4; ++i) { ex[(hw * 4 + i) * 64 + lane] = acc[i]; own[i] = acc[4 + i]; }
    asm volatile("; tiles 4..7" ::: "memory");
  } else {
#pragma unroll
    for (int i = 0; i < 4; ++i) { ex[(hw * 4 + i) * 64 + lane] = acc[4 + i]; own[i] = acc[i]; }
    asm volatile("; tiles 0..3" ::: "memory");
  }
  asm volatile("s_waitcnt lgkmcnt(0)" ::: "memory");
  if (lane == 0) __hip_atomic_fetch_add((LAS unsigned*)(sm + SY) + 6 + ch, 1u, __ATOMIC_RELAXED, __HIP_MEMORY_SCOPE_WORKGROUP);
  const float mult = rdlane(md.x, 0), dsk = rdlane(md.y, 0);
#pragma unroll
  for (int i = 0; i < 4; ++i) {
    const unsigned xr0 = xr[i].x, xr1 = xr[i].y;
    const f16x2 x0 = __builtin_bit_cast(f16x2, xr0), x1 = __builtin_bit_cast(f16x2, xr1);
    own[i] = own[i] * mult + (f32x4){(float)x0.x, (float)x0.y, (float)x1.x, (float)x1.y} * dsk;
    asm volatile("" : "+v"(own[i]));
  }
  lds_wait(sm, 6 + ch, 2u);
#pragma unroll
  for (int i = 0; i < 4; ++i) {
    const f32x4 y = ex[((hw ^ 1) * 4 + i) * 64 + lane] * mult + own[i];
    __builtin_amdgcn_raw_buffer_store_b64((v2u){pk_bf16(y[0], y[1]), pk_bf16(y[2], y[3])}, rs, WS_ST + d * 4096 + (t0 + 256 * i) * 2, 0, SC1);
  }
  __builtin_amdgcn_sched_barrier(0);
  w2_transpose(wr + 12); w2_transpose(wr + 16); w2_transpose(wr + 20);
  __builtin_amdgcn_sched_barrier(0);
  publish(rs, sm, 2, 4, WS_FS + b * FSTR, tag, lane);
  __builtin_amdgcn_s_setprio(0);
}

template <bool MAIN> DEV void gemm2(const Params& p, LAS char* sm, rsrc_t rs, int rt2, int ct2, int rl, bool isL, int w, int lane, v2u tag, v2u hf, v4u (&wr)[28]) {
  const int g = lane >> 4, hb = (lane >> 3) & 1, cb = lane & 7;
  {
    const int e8 = (w + rl) & 7;
    const unsigned need = (MAIN && w == 0) ? 0x3fffu : ((2u << ((7 * e8 + 6) >> 2)) - (1u << ((7 * e8) >> 2)));
    volatile LAS unsigned* c = (volatile LAS unsigned*)(sm + SY) + 5;
    if (MAIN && w == 0) {
#pragma unroll 1
      for (unsigned spins = 0; spins < SPIN_CAP; ++spins) {
        const unsigned m = (unsigned)__ballot(hf.x == tag.x && hf.y == tag.y) & 0x3fffu;
        *c = m;
        if (m == need) break;
        __builtin_amdgcn_s_sleep(2);
        asm volatile("" ::: "memory");
        hf = __builtin_amdgcn_raw_buffer_load_b64(rs, WS_FH + ((rt2 >> 1) * 16 + min(lane, 13)) * FSTR, 0, SC1);
      }
    } else {
#pragma unroll 1
      for (unsigned spins = 0; spins < 16 * SPIN_CAP && (__builtin_amdgcn_readfirstlane(*c) & need) != need; ++spins) __builtin_amdgcn_s_sleep(1);
    }
    __builtin_amdgcn_fence(__ATOMIC_ACQUIRE, "wavefront");
    asm volatile("" ::: "memory");
  }
  v4u ha[28];
  const int hoff = WS_HF + rt2 * 56 * 4096 + lane * 16;
  auto hload = [&](int i) { return __builtin_amdgcn_raw_buffer_load_b128(rs, hoff + (i & 3) * 1024, ks_of(w, rl, i >> 2) * 4096, SC1); };
#pragma unroll
  for (int i = 0; i < 16; ++i) ha[i] = hload(i);
  __builtin_amdgcn_sched_barrier(0);
#pragma unroll
  for (int ks = MAIN ? MAIN_PRE_T : 6; ks < 7; ++ks) { w2_transpose(wr + 4 * ks); __builtin_amdgcn_sched_barrier(0); }
  f32x4 acc[4][4] = {};
  auto mm = [&](int ks, int mt) {
#pragma unroll
    for (int t = 0; t < 4; ++t)
      acc[mt][t] = __builtin_amdgcn_mfma_f32_16x16x32_bf16(__builtin_bit_cast(bf16x8, ha[4 * ks + mt]), __builtin_bit_cast(bf16x8, wr[4 * ks + t]), acc[mt][t], 0, 0, 0);
  };
#pragma unroll
  for (int ks = 0; ks < 4; ++ks) {
#pragma unroll
    for (int mt = 0; mt < 4; ++mt) mm(ks, mt);
    __builtin_amdgcn_sched_barrier(0);
    if (ks < 2) {
#pragma unroll
      for (int i = 0; i < 6; ++i) { const int e = 6 * ks + i; ha[16 + 4 * (e % 3) + e / 3] = hload(16 + 4 * (e % 3) + e / 3); }
    }
    __builtin_amdgcn_sched_barrier(0);
  }
  lds_wait(sm, MAIN ? 1 : 2, MAIN && isL ? 0u : 4u);
  LAS f32x4* red = (LAS f32x4*)(sm + (w ^ 4) * 16384);
#pragma unroll
  for (int mt = 0; mt < 4; ++mt) {
#pragma unroll
    for (int ks = 4; ks < 7; ++ks) mm(ks, mt);
    if (mt > 0) {
#pragma unroll
      for (int t = 0; t < 4; ++t) red[((mt - 1) * 4 + t) * 64 + lane] = acc[mt - 1][t];
    }
#pragma unroll
    for (int i = 0; i < 4; ++i) { __builtin_amdgcn_sched_group_barrier(0x8, 3, 0); __builtin_amdgcn_sched_group_barrier(0x200, 1, 0); }
    __builtin_amdgcn_sched_barrier(0);
  }
#pragma unroll
  for (int t = 0; t < 4; ++t) red[(12 + t) * 64 + lane] = acc[3][t];
  const int tok = rt2 * 64 + 16 * w + 4 * g, chn = ct2 * 64 + 8 * cb + 4 * hb;
  v2u xv[4];
  f32x4 o[4], sf[4], xf[4];
  if (MAIN) {
#pragma unroll
    for (int i = 0; i < 4; ++i) xv[i] = *(const GAS v2u*)((const GAS char*)p.x + (size_t)(tok + i) * 1024 + chn * 2);
    if (w == 0) {
      lds_wait(sm, 2, 4u);
      wait_flags(rs, WS_FS + ct2 * 32 * FSTR, 32, tag, lane);
      lds_set(sm, 3);
    } else lds_wait(sm, 3);
#pragma unroll
    for (int i = 0; i < 2; ++i) {
      const int c = 16 * w + 8 * i + (lane >> 3);
      *(LAS v4u*)(sm + G2_S + c * 144 + (lane & 7) * 16) =
          __builtin_amdgcn_raw_buffer_load_b128(rs, WS_ST + (ct2 * 64 + c) * 4096 + rt2 * 128 + (lane & 7) * 16, 0, SC1);
    }
    wave_sync(sm, 10, 4);
#pragma unroll
    for (int t = 0; t < 4; ++t) {
      const LAS f32x4* r0 = (const LAS f32x4*)(sm + 65536) + (w * 4 + t) * 64 + lane;
      o[t] = (r0[0] + r0[1024]) + (r0[2048] + r0[3072]);
      const v2u sv = *(const LAS v2u*)(sm + G2_S + (8 * cb + 4 * hb + t) * 144 + 32 * w + 8 * g);
      sf[t] = (f32x4){bflo(sv.x), bfhi(sv.x), bflo(sv.y), bfhi(sv.y)};
      xf[t] = (f32x4){(t & 1) ? bfhi(xv[0][t >> 1]) : bflo(xv[0][t >> 1]), (t & 1) ? bfhi(xv[1][t >> 1]) : bflo(xv[1][t >> 1]),
                      (t & 1) ? bfhi(xv[2][t >> 1]) : bflo(xv[2][t >> 1]), (t & 1) ? bfhi(xv[3][t >> 1]) : bflo(xv[3][t >> 1])};
      asm volatile("" : "+v"(o[t]), "+v"(sf[t]), "+v"(xf[t]));
    }
  }
  lds_barrier();
  if (MAIN) {
    const rsrc_t ro = __builtin_amdgcn_make_buffer_rsrc(p.out, 0, L * D * 2, 0x00020000);
#pragma unroll
    for (int t = 0; t < 4; ++t) {
      const LAS f32x4* r0 = (const LAS f32x4*)sm + (w * 4 + t) * 64 + lane;
      o[t] += (r0[0] + r0[1024]) + (r0[2048] + r0[3072]);
      const f32x4 u = o[t] * -1.44269504088896f;
      const f32x4 dn = (f32x4){__builtin_amdgcn_exp2f(u[0]), __builtin_amdgcn_exp2f(u[1]), __builtin_amdgcn_exp2f(u[2]), __builtin_amdgcn_exp2f(u[3])} + 1.f;
      const f32x4 rc = {__builtin_amdgcn_rcpf(dn[0]), __builtin_amdgcn_rcpf(dn[1]), __builtin_amdgcn_rcpf(dn[2]), __builtin_amdgcn_rcpf(dn[3])};
      o[t] = __builtin_elementwise_fma(o[t] * rc, sf[t], xf[t]);
    }
#pragma unroll
    for (int k = 0; k < 2; ++k) {
      v4u r;
#pragma unroll
      for (int u = 0; u < 2; ++u) {
        const unsigned X = pk_bf16(o[2 * u][k], o[2 * u + 1][k]), Y = pk_bf16(o[2 * u][2 + k], o[2 * u + 1][2 + k]);
        r[u] = __builtin_amdgcn_update_dpp(X, Y, 0x128, 0xf, 0xc, false);
        r[2 + u] = __builtin_amdgcn_update_dpp(Y, X, 0x128, 0xf, 0x3, false);
      }
      __builtin_amdgcn_raw_buffer_store_b128(r, ro, (tok + 2 * hb + k) * 1024 + ct2 * 128 + cb * 16, 0, SC1);
    }
  }
}

__global__ void __launch_bounds__(512, 2) recurrent_scan__ssm_conv_s4d__dense_activation_silu__prompt_kernel(Params p) {
  __shared__ __attribute__((aligned(16))) char smem[LDS_BYTES];
  LAS char* sm = (LAS char*)smem;
  const int tid = threadIdx.x, lane = tid & 63, w = __builtin_amdgcn_readfirstlane(tid >> 6);
  const int b = blockIdx.x, xcd = b & 7, idx = b >> 3;
  const bool isL = idx < 4;
  const int ii = idx - 4, rt = (xcd & 3) * 4 + (ii & 3), ct = (xcd >> 2) * 7 + (ii >> 2);
  const int rt2 = (xcd & 3) * 8 + (xcd >> 2) * 4 + (idx >> 3), ct2 = idx & 7;
  const int rl = ((2 * (idx >> 3) + 4 * (ct2 & 1)) & 7) + 8 * ((xcd + 3 * ((ct2 >> 1) & 1)) % 7);
  const int t8 = (xcd & 3) * 64 + (xcd >> 2) * 32 + idx;
  unsigned pf;
  asm volatile("global_load_dword %0, %1, %2" : "=v"(pf) : "v"(min((((idx * 8 + w) ^ ((lane & 3) << 7)) << 7), CODE_BYTES - 128)), "s"(__builtin_amdgcn_s_getpc() & ~(u64)255));
  __builtin_amdgcn_sched_barrier(0);
  const rsrc_t rs = __builtin_amdgcn_make_buffer_rsrc(p.ws, 0, WS_BYTES, 0x00020000);
  asm volatile("" ::"s"(p.x), "s"(p.lar), "s"(p.aim), "s"(p.cre), "s"(p.cim), "s"(p.ldt), "s"(p.dsk), "s"(p.w1), "s"(p.w2), "s"(p.ws));
  v2u ep;
  KPar kp;
  v4u early[8];
  asm volatile("global_load_dwordx2 %0, %1, %2 sc1" : "=v"(ep) : "v"(WS_EPOCH), "s"(p.ws));
  if (w < 4) {
    const unsigned d2 = chan_of(b, w >> 1) * 2, o = d2 * NM + ((w & 1) * 64 + lane) * 2, o2 = o ^ 128;
    asm volatile("global_load_ushort %0, %8, %11\n\tglobal_load_ushort %1, %9, %12\n\tglobal_load_ushort %2, %9, %13\n\t"
                 "global_load_ushort %3, %9, %14\n\tglobal_load_ushort %4, %9, %15\n\tglobal_load_ushort %5, %10, %14\n\t"
                 "global_load_ushort %6, %10, %15\n\tglobal_load_ushort %7, %8, %16"
                 : "=&v"(kp.ldt), "=&v"(kp.lar), "=&v"(kp.aim), "=&v"(kp.c0), "=&v"(kp.c1), "=&v"(kp.c2), "=&v"(kp.c3), "=&v"(kp.dsk)
                 : "v"(d2), "v"(o), "v"(o2), "s"(p.ldt), "s"(p.lar), "s"(p.aim), "s"(p.cre), "s"(p.cim), "s"(p.dsk));
  }
  if (w == 6) {
    const char* sb = (const char*)p.x + (size_t)t8 * 8192;
    const unsigned o = lane * 16, o2 = o + 4096;
    asm volatile("global_load_dwordx4 %0, %8, %10\n\tglobal_load_dwordx4 %1, %8, %10 offset:1024\n\tglobal_load_dwordx4 %2, %8, %10 offset:2048\n\t"
                 "global_load_dwordx4 %3, %8, %10 offset:3072\n\tglobal_load_dwordx4 %4, %9, %10\n\tglobal_load_dwordx4 %5, %9, %10 offset:1024\n\t"
                 "global_load_dwordx4 %6, %9, %10 offset:2048\n\tglobal_load_dwordx4 %7, %9, %10 offset:3072"
                 : "=&v"(early[0]), "=&v"(early[1]), "=&v"(early[2]), "=&v"(early[3]), "=&v"(early[4]), "=&v"(early[5]), "=&v"(early[6]), "=&v"(early[7])
                 : "v"(o), "v"(o2), "s"(sb));
  }
  KIdx kix;
  if (w >= 4) kix = kexpand_idx(w - 4, lane);
  Loader ld;
  if (__builtin_expect((w & 6) == 4 && !isL, 1)) {
    ld = loader_init(p, sm, rt, ct, 2 * (ii & 3) + 2 * (ii >> 2), w - 4, lane);
    __builtin_amdgcn_s_sleep(LOADER_DELAY);
#pragma unroll 1
    for (int c = 0; c < 3; ++c) loader_issue(ld, c);
  }
  __builtin_amdgcn_sched_barrier(0);
  auto make_tag = [&]() {
    unsigned lo, hi;
    asm volatile("s_nop 0\n\tv_readfirstlane_b32 %0, %2\n\tv_readfirstlane_b32 %1, %3\n\ts_nop 1" : "=s"(lo), "=s"(hi) : "v"(ep.x), "v"(ep.y), "v"(pf));
    const u64 t = (((u64)hi << 32) | lo) + 1ull;
    return (v2u){(unsigned)t, (unsigned)(t >> 32)};
  };
  G1Base gb;
  if (__builtin_expect(w < 4, 1)) {
    __builtin_amdgcn_s_setprio(1);
    gb = g1_base(sm, w, lane);
    if (w == 0 && lane < 16) *(LAS unsigned*)(sm + SY + lane * 4) = 0u;
    if (w == 1 && lane < 8) *(LAS unsigned*)(sm + KG_K + (lane >> 2) * KG_KCH + (lane & 3) * 4) = 0u;
    __builtin_amdgcn_sched_barrier(0);
    drain();
    __builtin_amdgcn_sched_barrier(0);
    asm volatile("" : "+v"(kp.ldt), "+v"(kp.lar), "+v"(kp.aim), "+v"(kp.c0), "+v"(kp.c1), "+v"(kp.c2), "+v"(kp.c3), "+v"(kp.dsk));
    kgen(p, sm, kp, w, lane);
  }
  asm volatile("; roles" ::: "memory");
  v4u xt[8];
  if (__builtin_expect(w == 6, 1)) {
    drain();
    __builtin_amdgcn_sched_barrier(0);
    asm volatile("" : "+v"(early[0]), "+v"(early[1]), "+v"(early[2]), "+v"(early[3]), "+v"(early[4]), "+v"(early[5]), "+v"(early[6]), "+v"(early[7]));
    ln_wave(p, rs, t8, lane, make_tag(), early, xt);
  }
  asm volatile("; phases" ::: "memory");
  const rsrc_t rw = __builtin_amdgcn_make_buffer_rsrc((void*)p.w2, 0, F * D * 2, 0x00020000);
  const int woff = (16 * ((lane >> 3) & 1) + 4 * (lane >> 4)) * 1024 + ct2 * 128 + (lane & 7) * 16;
  if (w < 4) {
    const v2u tag = make_tag();
    if (isL) {
      asm volatile("s_waitcnt lgkmcnt(0)" ::: "memory");
#pragma unroll 1
      for (int i = 0; i < 7; ++i) bar();
      __builtin_amdgcn_s_setprio(0);
      lds_wait(sm, 4, 4u);
    } else g1_main(p, sm, rs, rt, ct, w, lane, tag, gb);
    v4u wr[28];
    w2_load<0, 28>(rw, woff, w, rl, wr);
    v2u hf = {0u, 0u};
    if (w == 0) hf = __builtin_amdgcn_raw_buffer_load_b64(rs, WS_FH + ((rt2 >> 1) * 16 + min(lane, 13)) * FSTR, 0, SC1);
    __builtin_amdgcn_sched_barrier(0);
#pragma unroll
    for (int ks = 0; ks < MAIN_PRE_T; ++ks) {
      w2_transpose(wr + 4 * ks);
      __builtin_amdgcn_sched_barrier(0);
    }
    gemm2<true>(p, sm, rs, rt2, ct2, rl, isL, w, lane, tag, hf, wr);
    __builtin_amdgcn_endpgm();
  } else {
    v2u tag, f[4];
    v4u xv[8];
    int poff, pst1, pst2;
    if (w >= 6) {
      if (w == 7) drain();
      __builtin_amdgcn_sched_barrier(0);
      tag = make_tag();
      if (w == 6) {
        bar();
#pragma unroll
        for (int j = 0; j < 8; ++j) __builtin_amdgcn_raw_buffer_store_b128(xt[j], rs, WS_XT + t8 * 8192 + j * 1024 + lane * 16, 0, SC1);
        if (!isL) { bar(); bar(); }
        drain();
        if (lane == 0) __builtin_amdgcn_raw_buffer_store_b64(tag, rs, WS_FX + b * FXS, 0, SC1);
#pragma unroll 1
        for (int i = isL ? 1 : 3; i < 6; ++i) bar();
      } else {
#pragma unroll 1
        for (int i = 0; i < 6; ++i) bar();
      }
      poff = w == 7 ? WS_FX + lane * FXS : WS_GS + (isL ? 0 : rt * 1024) + lane * 8;
      pst1 = w == 7 ? 64 * FXS : 512;
      pst2 = w == 7 ? 128 * FXS : WS_GC - WS_GS + (isL ? 0 : (ct - rt) * 1024);
      if (!isL) {
#pragma unroll
        for (int i = 0; i < 4; ++i) f[i] = __builtin_amdgcn_raw_buffer_load_b64(rs, poff, (i & 1) * pst1 + (i >> 1) * pst2, SC1);
      }
    } else if (isL) {
      const int cg = (xcd & 3) * 4 + idx;
      cs_part(p, sm, (xcd >> 2) * 14 + min(cg, 13), w - 4, lane);
      lds_barrier();
      if (w == 4 && cg < 14) {
        const float cs = *(const LAS float*)(sm + 65536 + lane * 4) + *(const LAS float*)(sm + 65536 + 256 + lane * 4);
        __builtin_amdgcn_raw_buffer_store_b64((v2u){__float_as_uint(cs), make_tag().x}, rs, WS_GC + (((xcd >> 2) * 14 + cg) * 64 + lane) * 8, 0, SC1);
      }
#pragma unroll 1
      for (int i = 0; i < 5; ++i) bar();
    }
    __builtin_amdgcn_sched_barrier(0);
    v4u wr[28];
    w2_load<0, W2_A>(rw, woff, w, rl, wr);
    __builtin_amdgcn_sched_barrier(0);
    if (w < 6 && !isL) {
#pragma unroll 1
      for (int c = 2; c < 8; ++c) {
        if (c > 2) loader_issue(ld, c);
        if (c < 5) asm volatile("s_waitcnt vmcnt(%0)" ::"n"(32 + W2_A) : "memory");
        else asm volatile("s_waitcnt vmcnt(32)" ::: "memory");
        __builtin_amdgcn_s_barrier();
      }
    }
    __builtin_amdgcn_sched_barrier(0);
    w2_load<W2_A, W2_B>(rw, woff, w, rl, wr);
    __builtin_amdgcn_sched_barrier(0);
    if (w < 6) asm volatile("s_waitcnt vmcnt(%0)" ::"n"(W2_B - W2_A) : "memory");
    bar();
    auto poll4 = [&](unsigned tx, unsigned mx) {
#pragma unroll 1
      for (unsigned spins = 0; spins < SPIN_CAP; ++spins) {
        asm volatile("" ::: "memory");
#pragma unroll
        for (int i = 0; i < 4; ++i) f[i] = __builtin_amdgcn_raw_buffer_load_b64(rs, poff, (i & 1) * pst1 + (i >> 1) * pst2, SC1);
        unsigned bad = 0;
#pragma unroll
        for (int i = 0; i < 4; ++i) bad |= (f[i].y ^ tx) | ((f[i].x ^ tag.x) & mx);
        if (__all(bad == 0u)) break;
        __builtin_amdgcn_s_sleep(12);
      }
      __builtin_amdgcn_fence(__ATOMIC_ACQUIRE, "wavefront");
    };
    auto load_x = [&](v4u (&y)[8]) {
#pragma unroll
      for (int i = 0; i < 8; ++i)
        y[i] = __builtin_amdgcn_raw_buffer_load_b128(rs, WS_XT + (lane >> 1) * 8192 + (b & 7) * 1024 + (2 * (b >> 3) + (lane & 1)) * 16, i * 32 * 8192, SC1);
    };
    bool have_x = false;
    if (w < 6) tag = make_tag();
    else if (!isL) {
      if (w == 6) {
        if (!__all(((f[0].y ^ tag.x) | (f[1].y ^ tag.x) | (f[2].y ^ tag.x) | (f[3].y ^ tag.x)) == 0u)) poll4(tag.x, 0u);
#pragma unroll
        for (int i = 0; i < 4; ++i) *(LAS unsigned*)(sm + GR + i * 256 + lane * 4) = f[i].x;
        asm volatile("" ::: "memory");
        lds_set(sm, 8);
      } else {
        unsigned bad = 0;
#pragma unroll
        for (int i = 0; i < 4; ++i) bad |= (f[i].y ^ tag.y) | (f[i].x ^ tag.x);
        have_x = __all(bad == 0u);
        __builtin_amdgcn_fence(__ATOMIC_ACQUIRE, "wavefront");
        if (have_x) load_x(xv);
      }
    }
    __builtin_amdgcn_sched_barrier(0);
    w2_load<W2_B, 28>(rw, woff, w, rl, wr);
    __builtin_amdgcn_sched_barrier(0);
    __builtin_amdgcn_sched_barrier(0);
    __builtin_amdgcn_s_setprio(EXP_PRIO);
    kexpand(sm, w - 4, lane, kix);
    __builtin_amdgcn_sched_barrier(0);
    auto stage_x = [&](const v4u (&y)[8]) {
#pragma unroll
      for (int i = 0; i < 8; ++i) *(LAS v4u*)(sm + CV_X + (lane & 1) * CV_XS + 512 + (i * 32 + (lane >> 1)) * 16) = y[i];
    };
    if (have_x) { stage_x(xv); asm volatile("; early x" ::: "memory"); }
    else if (w == 7) {
      v4u y[8];
      poll4(tag.y, ~0u);
      load_x(y);
      if (b == 0 && lane == 0) __builtin_amdgcn_raw_buffer_store_b64(tag, rs, WS_EPOCH, 0, SC1);
      stage_x(y);
      asm volatile("; late x" ::: "memory");
    }
    __builtin_amdgcn_s_setprio(0);
    asm volatile("s_waitcnt lgkmcnt(0)" ::: "memory");
    if (lane == 0) __hip_atomic_fetch_add((LAS unsigned*)(sm + SY) + 4, 1u, __ATOMIC_RELAXED, __HIP_MEMORY_SCOPE_WORKGROUP);
    __builtin_amdgcn_sched_barrier(0);
    w2_transpose(wr); w2_transpose(wr + 4); w2_transpose(wr + 8);
    __builtin_amdgcn_sched_barrier(0);
    if (!isL) lds_wait(sm, 8 + w);
    conv(p, sm, rs, b, w - 4, lane, tag, wr);
    gemm2<false>(p, sm, rs, rt2, ct2, rl, isL, w, lane, tag, tag, wr);
    __builtin_amdgcn_endpgm();
  }
}

extern "C" void kernel_launch(void* const* d_in, const int* in_sizes, int n_in, void* d_out, int out_size, void* d_ws, size_t ws_size,
                              hipStream_t stream) {
  Params p;
  p.x = (const u16*)d_in[0]; p.lar = (const u16*)d_in[1]; p.aim = (const u16*)d_in[2]; p.cre = (const u16*)d_in[3];
  p.cim = (const u16*)d_in[4]; p.ldt = (const u16*)d_in[5]; p.dsk = (const u16*)d_in[6]; p.w1 = (const u16*)d_in[8];
  p.w2 = (const u16*)d_in[9]; p.out = (u16*)d_out; p.ws = (char*)d_ws;
  recurrent_scan__ssm_conv_s4d__dense_activation_silu__prompt_kernel<<<256, 512, 0, stream>>>(p);
}
```
